```python
import torch

def _softmax(x, axis=-1):
    return torch.softmax(x, dim=axis)
def _gelu(x):
    return torch.nn.functional.gelu(x, approximate="tanh")
def _silu(x):
    return torch.nn.functional.silu(x)
def _sigmoid(x):
    return torch.sigmoid(x)
def _layernorm(x, axis=-1, eps=1e-5):
    m = x.mean(dim=axis, keepdim=True)
    v = x.var(dim=axis, keepdim=True, unbiased=False)
    return (x - m) / torch.sqrt(v + eps)
def _scatter_add(tgt, idx, src, axis=0):
    return tgt.clone().index_add_(axis, idx, src)
def _segment_sum(src, seg, n):
    out = torch.zeros((n,) + src.shape[1:], dtype=src.dtype)
    return out.index_add_(0, seg, src)
def _pad(x, pw):
    flat = tuple(v for pair in reversed(pw) for v in pair)
    return torch.nn.functional.pad(x, flat)

kernel_name = 'sequential_preLN__gqa_block_cached__geglu_ffn__decode'

def setup_inputs():

    _w = (lambda a: (lambda t: t.to(torch.bfloat16) if t.is_floating_point() else t)(torch.as_tensor(a)))
    def _A_setup():
        B, Sq, D = 2, 1, 640
        Hq, Hkv, Sk, Dh = 32, 32, 8192, 128
        assert Hq % Hkv == 0, (Hq, Hkv)
        return {
            "x": _w((lambda *s: torch.randn(*s, dtype=torch.bfloat16) * 0.05)(B, Sq, D)),
            "k_cache": _w((lambda *s: torch.randn(*s, dtype=torch.bfloat16) * 3.2)(B, Hkv, Sk, Dh)),
            "v_cache": _w((lambda *s: torch.randn(*s, dtype=torch.bfloat16) * 0.4)(B, Hkv, Sk, Dh)),
            "wq": _w((lambda *s: torch.randn(*s, dtype=torch.bfloat16) * 3.2)(D, Hq * Dh) * (D ** -0.5)),
            "wo": _w((lambda *s: torch.randn(*s, dtype=torch.bfloat16) * 0.4)(Hq * Dh, D) * ((Hq * Dh) ** -0.5)),
        }
    _A_in = _A_setup()

    def _B_setup():
        B, S, D, F = 2, 1, 640, 1728
        return {
            "x": _w((lambda *s: torch.randn(*s, dtype=torch.bfloat16) * 0.05)(B, S, D)),
            "w_gate": _w((lambda *s: torch.randn(*s, dtype=torch.bfloat16) * 0.05)(D, F)),
            "w_up": _w((lambda *s: torch.randn(*s, dtype=torch.bfloat16) * 0.05)(D, F)),
            "w_down": _w((lambda *s: torch.randn(*s, dtype=torch.bfloat16) * 0.05)(F, D)),
        }
    _B_in = _B_setup()

    return {
        "A_x": _A_in["x"],
        "A_k_cache": _A_in["k_cache"],
        "A_v_cache": _A_in["v_cache"],
        "A_wq": _A_in["wq"],
        "A_wo": _A_in["wo"],
        "B_x": _B_in["x"],
        "B_w_gate": _B_in["w_gate"],
        "B_w_up": _B_in["w_up"],
        "B_w_down": _B_in["w_down"],
    }

def reference(A_x, A_k_cache, A_v_cache, A_wq, A_wo, B_x, B_w_gate, B_w_up, B_w_down):
    def _A_ref(x, k_cache, v_cache, wq, wo):
        B, Sq, D = x.shape
        Hq, Dh = 32, 128
        Hkv = k_cache.shape[1]
        rep = Hq // Hkv
        q = (lambda x, *d: x.permute(*d))((x @ wq).reshape(B, Sq, Hq, Dh), 0, 2, 1, 3)
        k = (lambda x, n, axis: torch.repeat_interleave(x, n, dim=axis))(k_cache, rep, axis=1)
        v = (lambda x, n, axis: torch.repeat_interleave(x, n, dim=axis))(v_cache, rep, axis=1)
        scores = torch.einsum("bhqd,bhkd->bhqk", q, k) * (Dh ** -0.5)
        p = _softmax(scores, axis=-1)
        o = torch.einsum("bhqk,bhkd->bhqd", p, v)
        return (lambda x, *d: x.permute(*d))(o, 0, 2, 1, 3).reshape(B, Sq, Hq * Dh) @ wo

    def _B_ref(x, w_gate, w_up, w_down):
        return (_gelu(x @ w_gate) * (x @ w_up)) @ w_down

    x = A_x
    h = x + _A_ref(_layernorm(x, axis=-1), A_k_cache, A_v_cache, A_wq, A_wo)
    h2 = h + _B_ref(_layernorm(h, axis=-1), B_w_gate, B_w_up, B_w_down)
    return h2
```

```cpp
#include <hip/hip_runtime.h>
#include <hip/hip_bf16.h>
#include <stdint.h>

#define MIMIC 1

typedef unsigned u32x4 __attribute__((ext_vector_type(4)));
typedef __bf16 bf16x2 __attribute__((ext_vector_type(2)));
typedef unsigned short u16;

constexpr int B = 2, D = 640, H = 32, SK = 8192, DH = 128, F = 1728, HD = H * DH;
constexpr int NSPLIT = 4, KEYS = SK / NSPLIT;

__device__ __forceinline__ float bf2f(u16 v) { return __uint_as_float((unsigned)v << 16); }
__device__ __forceinline__ float rbf(float f) {
  __bf16 h = (__bf16)f; return (float)h;
}
__device__ __forceinline__ float rb(float f) { return MIMIC ? rbf(f) : f; }
__device__ __forceinline__ u16 f2bf(float f) { __bf16 h = (__bf16)f; return __builtin_bit_cast(u16, h); }
__device__ __forceinline__ float dot2(unsigned a, unsigned b, float c) {
  return __builtin_amdgcn_fdot2_f32_bf16(__builtin_bit_cast(bf16x2, a), __builtin_bit_cast(bf16x2, b), c, false);
}
template <int CTRL> __device__ __forceinline__ float dpp(float x) {
  return __builtin_bit_cast(float, __builtin_amdgcn_mov_dpp(__builtin_bit_cast(int, x), CTRL, 0xf, 0xf, true));
}
__device__ __forceinline__ float row_sum16(float x) {
  x += dpp<0x128>(x); x += dpp<0x124>(x); x += dpp<0x122>(x); x += dpp<0x121>(x); return x;
}
__device__ __forceinline__ float wave_sum(float x) {
  for (int o = 32; o; o >>= 1) x += __shfl_xor(x, o);
  return x;
}

__device__ void block_ln(const float* xin  , float* xn  , float* red) {
  int lane = threadIdx.x & 63;
  float s = 0.f;
  for (int i = lane; i < D; i += 64) s += xin[i];
  s = wave_sum(s);
  float mean = s / D;
  float v = 0.f;
  for (int i = lane; i < D; i += 64) { float d = xin[i] - mean; v += d * d; }
  v = wave_sum(v) / D;
  float mb = rb(mean), vb = rb(v);
  float den = rb(sqrtf(rb(vb + 1e-5f)));
  for (int i = threadIdx.x; i < D; i += blockDim.x) xn[i] = rb(rb(xin[i] - mb) / den);
}

__global__ void __launch_bounds__(256) k_q(const u16* x, const u16* wq, u16* q) {
  __shared__ float xin[2][D], xn[2][D], red[4][2][64];
  for (int i = threadIdx.x; i < 2 * D; i += 256) xin[i / D][i % D] = bf2f(x[i]);
  __syncthreads();
  block_ln(xin[0], xn[0], nullptr); block_ln(xin[1], xn[1], nullptr);
  __syncthreads();
  int col = blockIdx.x * 64 + (threadIdx.x & 63), w = threadIdx.x >> 6;
  float a0 = 0, a1 = 0;
  for (int k = w; k < D; k += 4) { float wv = bf2f(wq[(size_t)k * HD + col]); a0 += xn[0][k] * wv; a1 += xn[1][k] * wv; }
  red[w][0][threadIdx.x & 63] = a0; red[w][1][threadIdx.x & 63] = a1;
  __syncthreads();
  if (threadIdx.x < 128) { int b = threadIdx.x >> 6, c = threadIdx.x & 63;
    float s = red[0][b][c] + red[1][b][c] + red[2][b][c] + red[3][b][c];
    q[b * HD + blockIdx.x * 64 + c] = f2bf(s); }
}

constexpr int AW = 8;
__global__ void __launch_bounds__(512) k_attn(const u16* q, const u16* kc, const u16* vc, float* part) {
  __shared__ float mrg[AW * 4][132];
  int wg = blockIdx.x, bh = wg / NSPLIT, s = wg % NSPLIT;
  int lane = threadIdx.x & 63, w = threadIdx.x >> 6, r = lane >> 4, c = lane & 15;
  u32x4 qv = *(const u32x4*)(q + bh * DH + 8 * c);
  const u16* kb = kc + ((size_t)bh * SK + s * KEYS + w * 16 + r) * DH + 8 * c;
  const u16* vb = vc + ((size_t)bh * SK + s * KEYS + w * 16 + r) * DH + 8 * c;
  float m = -INFINITY, l = 0.f, acc[8] = {0, 0, 0, 0, 0, 0, 0, 0};
  constexpr int NIT = KEYS / (16 * AW);
  u32x4 Kn[4], Vn[4];
#pragma unroll
  for (int t = 0; t < 4; ++t) { Kn[t] = __builtin_nontemporal_load((const u32x4*)(kb + t * 4 * DH)); Vn[t] = __builtin_nontemporal_load((const u32x4*)(vb + t * 4 * DH)); }
  for (int it = 0; it < NIT; ++it) {
    u32x4 Kc[4], Vc[4];
#pragma unroll
    for (int t = 0; t < 4; ++t) { Kc[t] = Kn[t]; Vc[t] = Vn[t]; }
    if (it + 1 < NIT) {
      const u16* k2 = kb + (size_t)(it + 1) * 16 * AW * DH; const u16* v2 = vb + (size_t)(it + 1) * 16 * AW * DH;
#pragma unroll
      for (int t = 0; t < 4; ++t) { Kn[t] = __builtin_nontemporal_load((const u32x4*)(k2 + t * 4 * DH)); Vn[t] = __builtin_nontemporal_load((const u32x4*)(v2 + t * 4 * DH)); }
    }
    float sc[4];
#pragma unroll
    for (int t = 0; t < 4; ++t) { float d = 0.f;
#pragma unroll
      for (int k = 0; k < 4; ++k) d = dot2(Kc[t][k], qv[k], d);
      d = row_sum16(d);
      sc[t] = rb(rb(d) * 0.08838834764831845f) * 1.4426950408889634f; }
    float mn = fmaxf(fmaxf(fmaxf(sc[0], sc[1]), fmaxf(sc[2], sc[3])), m);
    float al = __builtin_amdgcn_exp2f(m - mn); m = mn;
    l *= al;
#pragma unroll
    for (int k = 0; k < 8; ++k) acc[k] *= al;
#pragma unroll
    for (int t = 0; t < 4; ++t) { float p = __builtin_amdgcn_exp2f(sc[t] - mn); l += p;
#pragma unroll
      for (int k = 0; k < 4; ++k) { acc[2 * k] += p * __uint_as_float(Vc[t][k] << 16); acc[2 * k + 1] += p * __uint_as_float(Vc[t][k] & 0xffff0000u); } }
  }
  float* rec = mrg[w * 4 + r];
#pragma unroll
  for (int k = 0; k < 8; ++k) rec[4 + 8 * c + k] = acc[k];
  if (c == 0) { rec[0] = m; rec[1] = l; }
  __syncthreads();
  if (threadIdx.x < 128) {
    float M = -INFINITY;
    for (int j = 0; j < AW * 4; ++j) M = fmaxf(M, mrg[j][0]);
    float L = 0.f, o = 0.f;
    for (int j = 0; j < AW * 4; ++j) { float wt = __builtin_amdgcn_exp2f(mrg[j][0] - M); L += wt * mrg[j][1]; o += wt * mrg[j][4 + threadIdx.x]; }
    part[wg * 132 + 4 + threadIdx.x] = o;
    if (threadIdx.x == 0) { part[wg * 132] = M; part[wg * 132 + 1] = L; }
  }
}

__global__ void __launch_bounds__(256) k_wo(const float* part, const u16* wo, float* hp) {
  __shared__ float o[2][256], red[4][2][64];
  int cg = blockIdx.x, kg = blockIdx.y;
  for (int i = threadIdx.x; i < 512; i += 256) { int b = i >> 8, kk = i & 255, row = kg * 256 + kk, h = row >> 7, d = row & 127;
    const float* p = part + (size_t)((b * H + h) * NSPLIT) * 132;
    float M = -INFINITY; for (int s = 0; s < NSPLIT; ++s) M = fmaxf(M, p[s * 132]);
    float L = 0, a = 0; for (int s = 0; s < NSPLIT; ++s) { float wt = __builtin_amdgcn_exp2f(p[s * 132] - M); L += wt * p[s * 132 + 1]; a += wt * p[s * 132 + 4 + d]; }
    o[b][kk] = rb(a / L); }
  __syncthreads();
  int c = threadIdx.x & 63, w = threadIdx.x >> 6, col = cg * 64 + c;
  float a0 = 0, a1 = 0;
  for (int k = w; k < 256; k += 4) { float wv = bf2f(wo[(size_t)(kg * 256 + k) * D + col]); a0 += o[0][k] * wv; a1 += o[1][k] * wv; }
  red[w][0][c] = a0; red[w][1][c] = a1;
  __syncthreads();
  if (threadIdx.x < 128) { int b = threadIdx.x >> 6; hp[(kg * 2 + b) * D + cg * 64 + c] = red[0][b][c] + red[1][b][c] + red[2][b][c] + red[3][b][c]; }
}

__global__ void __launch_bounds__(256) k_gu(const u16* x, const float* hp, const u16* wg_, const u16* wu, float* hbuf, float* g) {
  __shared__ float xin[2][D], xn[2][D], red[4][4][64];
  for (int i = threadIdx.x; i < 2 * D; i += 256) { int b = i / D, d = i % D; float s = 0; for (int k = 0; k < 16; ++k) s += hp[(k * 2 + b) * D + d];
    float h = rb(bf2f(x[i]) + rb(s)); xin[b][d] = h; if (blockIdx.x == 0) hbuf[i] = h; }
  __syncthreads();
  block_ln(xin[0], xn[0], nullptr); block_ln(xin[1], xn[1], nullptr);
  __syncthreads();
  int c = threadIdx.x & 63, w = threadIdx.x >> 6, col = blockIdx.x * 64 + c;
  float g0 = 0, g1 = 0, u0 = 0, u1 = 0;
  for (int k = w; k < D; k += 4) { float a = bf2f(wg_[(size_t)k * F + col]), b = bf2f(wu[(size_t)k * F + col]);
    g0 += xn[0][k] * a; g1 += xn[1][k] * a; u0 += xn[0][k] * b; u1 += xn[1][k] * b; }
  red[w][0][c] = g0; red[w][1][c] = g1; red[w][2][c] = u0; red[w][3][c] = u1;
  __syncthreads();
  if (threadIdx.x < 128) { int b = threadIdx.x >> 6;
    float gg = rb(red[0][b][c] + red[1][b][c] + red[2][b][c] + red[3][b][c]);
    float uu = rb(red[0][2 + b][c] + red[1][2 + b][c] + red[2][2 + b][c] + red[3][2 + b][c]);
    float t = 0.7978845608028654f * (gg + 0.044715f * gg * gg * gg);
    float ge = rb(0.5f * gg * (1.f + tanhf(t)));
    g[b * F + blockIdx.x * 64 + c] = rb(ge * uu); }
}

__global__ void __launch_bounds__(1024) k_dn(const float* hbuf, const float* g, const u16* wd, u16* out) {
  __shared__ float gs[2][F], red[16][2][64];
  for (int i = threadIdx.x; i < 2 * F; i += 1024) gs[i / F][i % F] = g[i];
  __syncthreads();
  int c = threadIdx.x & 63, w = threadIdx.x >> 6, col = blockIdx.x * 64 + c;
  float a0 = 0, a1 = 0;
  for (int k = w; k < F; k += 16) { float wv = bf2f(wd[(size_t)k * D + col]); a0 += gs[0][k] * wv; a1 += gs[1][k] * wv; }
  red[w][0][c] = a0; red[w][1][c] = a1;
  __syncthreads();
  if (threadIdx.x < 128) { int b = threadIdx.x >> 6; float s = 0; for (int j = 0; j < 16; ++j) s += red[j][b][c];
    out[b * D + col] = f2bf(hbuf[b * D + col] + rb(s)); }
}

extern "C" void kernel_launch(void* const* d_in, const int* in_sizes, int n_in, void* d_out, int out_size, void* d_ws, size_t ws_size, hipStream_t stream) {
  const u16* x = (const u16*)d_in[0]; const u16* kc = (const u16*)d_in[1]; const u16* vc = (const u16*)d_in[2];
  const u16* wq = (const u16*)d_in[3]; const u16* wo = (const u16*)d_in[4];
  const u16* wg = (const u16*)d_in[6]; const u16* wu = (const u16*)d_in[7]; const u16* wd = (const u16*)d_in[8];
  char* ws = (char*)d_ws;
  u16* q = (u16*)ws; float* part = (float*)(ws + 65536); float* hp = (float*)(ws + 65536 * 4); float* hbuf = (float*)(ws + 65536 * 6); float* g = (float*)(ws + 65536 * 7);
  k_q<<<64, 256, 0, stream>>>(x, wq, q);
  k_attn<<<256, 512, 0, stream>>>(q, kc, vc, part);
  k_wo<<<dim3(10, 16), 256, 0, stream>>>(part, wo, hp);
  k_gu<<<27, 256, 0, stream>>>(x, hp, wg, wu, hbuf, g);
  k_dn<<<10, 1024, 0, stream>>>(hbuf, g, wd, (u16*)d_out);
}
```

```cpp
#include <hip/hip_runtime.h>
#include <hip/hip_bf16.h>
#include <stdint.h>
#define WITH_MEMSET 0
#define EXTRA_EMPTY 0
typedef unsigned u32x4 __attribute__((ext_vector_type(4)));
typedef __bf16 bf16x2 __attribute__((ext_vector_type(2)));
typedef unsigned short u16;
constexpr int B = 2, D = 640, H = 32, SK = 8192, DH = 128, F = 1728, HD = H * DH;
constexpr int NSPLIT = 4, KEYS = SK / NSPLIT;
__device__ __forceinline__ float bf2f(u16 v) { return __uint_as_float((unsigned)v << 16); }
__device__ __forceinline__ float rb(float f) { __bf16 h = (__bf16)f; return (float)h; }
__device__ __forceinline__ u16 f2bf(float f) { __bf16 h = (__bf16)f; return __builtin_bit_cast(u16, h); }
__device__ __forceinline__ float lo(unsigned v) { return __uint_as_float(v << 16); }
__device__ __forceinline__ float hi(unsigned v) { return __uint_as_float(v & 0xffff0000u); }
__device__ __forceinline__ float dot2(unsigned a, unsigned b, float c) {
  return __builtin_amdgcn_fdot2_f32_bf16(__builtin_bit_cast(bf16x2, a), __builtin_bit_cast(bf16x2, b), c, false);
}
template <int CTRL> __device__ __forceinline__ float dpp(float x) {
  return __builtin_bit_cast(float, __builtin_amdgcn_mov_dpp(__builtin_bit_cast(int, x), CTRL, 0xf, 0xf, true));
}
__device__ __forceinline__ float row_sum16(float x) { x += dpp<0x128>(x); x += dpp<0x124>(x); x += dpp<0x122>(x); x += dpp<0x121>(x); return x; }
__device__ __forceinline__ float wave_sum(float x) { for (int o = 32; o; o >>= 1) x += __shfl_xor(x, o); return x; }
__device__ __forceinline__ u32x4 ldnt(const void* p) { return __builtin_nontemporal_load((const u32x4*)p); }

__device__ void block_ln(const float* xin, float* xn) {
  int lane = threadIdx.x & 63;
  float s = 0.f;
  for (int i = lane; i < D; i += 64) s += xin[i];
  s = wave_sum(s);
  float mean = s / D, v = 0.f;
  for (int i = lane; i < D; i += 64) { float d = xin[i] - mean; v += d * d; }
  v = wave_sum(v) / D;
  float mb = rb(mean), vb = rb(v), den = rb(sqrtf(rb(vb + 1e-5f)));
  for (int i = threadIdx.x; i < D; i += blockDim.x) xn[i] = rb(rb(xin[i] - mb) / den);
}

__global__ void __launch_bounds__(256) k_q(const u16* x, const u16* wq, u16* q) {
  __shared__ float xin[2][D], xn[2][D], red[32][2][65];
  int cl = threadIdx.x & 7, rl = threadIdx.x >> 3;
  u32x4 w[20];
#pragma unroll
  for (int i = 0; i < 20; ++i) w[i] = ldnt(wq + (size_t)(rl + 32 * i) * HD + blockIdx.x * 64 + cl * 8);
  for (int i = threadIdx.x; i < 2 * D; i += 256) xin[i / D][i % D] = bf2f(x[i]);
  __syncthreads();
  block_ln(xin[0], xn[0]); block_ln(xin[1], xn[1]);
  __syncthreads();
  float a[2][8] = {};
#pragma unroll
  for (int i = 0; i < 20; ++i) { float x0 = xn[0][rl + 32 * i], x1 = xn[1][rl + 32 * i];
#pragma unroll
    for (int k = 0; k < 4; ++k) { a[0][2 * k] += x0 * lo(w[i][k]); a[0][2 * k + 1] += x0 * hi(w[i][k]); a[1][2 * k] += x1 * lo(w[i][k]); a[1][2 * k + 1] += x1 * hi(w[i][k]); } }
#pragma unroll
  for (int k = 0; k < 8; ++k) { red[rl][0][cl * 8 + k] = a[0][k]; red[rl][1][cl * 8 + k] = a[1][k]; }
  __syncthreads();
  if (threadIdx.x < 128) { int b = threadIdx.x >> 6, c = threadIdx.x & 63; float s = 0; for (int j = 0; j < 32; ++j) s += red[j][b][c]; q[b * HD + blockIdx.x * 64 + c] = f2bf(s); }
}

constexpr int AW = 8;
__device__ __forceinline__ void attn_step(const u32x4 (&Kc)[4], const u32x4 (&Vc)[4], const u32x4& qv, float& m, float& l, float (&acc)[8]) {
  float sc[4];
#pragma unroll
  for (int t = 0; t < 4; ++t) { float d = 0.f;
#pragma unroll
    for (int k = 0; k < 4; ++k) d = dot2(Kc[t][k], qv[k], d);
    d = row_sum16(d);
    sc[t] = rb(rb(d) * 0.08838834764831845f) * 1.4426950408889634f; }
  float mn = fmaxf(fmaxf(fmaxf(sc[0], sc[1]), fmaxf(sc[2], sc[3])), m);
  float al = __builtin_amdgcn_exp2f(m - mn); m = mn;
  l *= al;
#pragma unroll
  for (int k = 0; k < 8; ++k) acc[k] *= al;
#pragma unroll
  for (int t = 0; t < 4; ++t) { float p = __builtin_amdgcn_exp2f(sc[t] - mn); l += p;
#pragma unroll
    for (int k = 0; k < 4; ++k) { acc[2 * k] += p * lo(Vc[t][k]); acc[2 * k + 1] += p * hi(Vc[t][k]); } }
}
__global__ void __launch_bounds__(512) k_attn(const u16* q, const u16* kc, const u16* vc, float* part) {
  __shared__ float mrg[AW * 4][132];
  int wg = blockIdx.x, bh = wg / NSPLIT, s = wg % NSPLIT;
  int lane = threadIdx.x & 63, w = __builtin_amdgcn_readfirstlane(threadIdx.x >> 6), r = lane >> 4, c = lane & 15;
  u32x4 qv = *(const u32x4*)(q + bh * DH + 8 * c);
  const u16* kb = kc + ((size_t)bh * SK + s * KEYS + w * 16 + r) * DH + 8 * c;
  const u16* vb = vc + ((size_t)bh * SK + s * KEYS + w * 16 + r) * DH + 8 * c;
  float m = -INFINITY, l = 0.f, acc[8] = {0, 0, 0, 0, 0, 0, 0, 0};
  constexpr int NIT = KEYS / (16 * AW), STEP = 16 * AW * DH, DP = 2;
  u32x4 Kb[DP][4], Vb[DP][4];
#pragma unroll
  for (int d = 0; d < DP; ++d) {
#pragma unroll
    for (int t = 0; t < 4; ++t) Kb[d][t] = ldnt(kb + d * STEP + t * 4 * DH);
#pragma unroll
    for (int t = 0; t < 4; ++t) Vb[d][t] = ldnt(vb + d * STEP + t * 4 * DH);
    __builtin_amdgcn_sched_barrier(0);
  }
  kb += DP * STEP; vb += DP * STEP;
#pragma unroll 1
  for (int it = 0; it < NIT / DP - 1; ++it) {
#pragma unroll
    for (int d = 0; d < DP; ++d) {
      attn_step(Kb[d], Vb[d], qv, m, l, acc);
      __builtin_amdgcn_sched_barrier(0);
#pragma unroll
      for (int t = 0; t < 4; ++t) Kb[d][t] = ldnt(kb + d * STEP + t * 4 * DH);
#pragma unroll
      for (int t = 0; t < 4; ++t) Vb[d][t] = ldnt(vb + d * STEP + t * 4 * DH);
      __builtin_amdgcn_sched_barrier(0);
    }
    kb += DP * STEP; vb += DP * STEP;
  }
#pragma unroll
  for (int d = 0; d < DP; ++d) attn_step(Kb[d], Vb[d], qv, m, l, acc);
  float* rec = mrg[w * 4 + r];
#pragma unroll
  for (int k = 0; k < 8; ++k) rec[4 + 8 * c + k] = acc[k];
  if (c == 0) { rec[0] = m; rec[1] = l; }
  __syncthreads();
  if (threadIdx.x < 128) {
    float M = -INFINITY;
    for (int j = 0; j < AW * 4; ++j) M = fmaxf(M, mrg[j][0]);
    float L = 0.f, o = 0.f;
    for (int j = 0; j < AW * 4; ++j) { float wt = __builtin_amdgcn_exp2f(mrg[j][0] - M); L += wt * mrg[j][1]; o += wt * mrg[j][4 + threadIdx.x]; }
    part[wg * 132 + 4 + threadIdx.x] = o;
    if (threadIdx.x == 0) { part[wg * 132] = M; part[wg * 132 + 1] = L; }
  }
}

__global__ void __launch_bounds__(256) k_wo(const float* part, const u16* wo, float* hp) {
  __shared__ float o[2][256], red[32][2][65];
  int cg = blockIdx.x, kg = blockIdx.y, cl = threadIdx.x & 7, rl = threadIdx.x >> 3;
  u32x4 w[8];
#pragma unroll
  for (int i = 0; i < 8; ++i) w[i] = ldnt(wo + (size_t)(kg * 256 + rl + 32 * i) * D + cg * 64 + cl * 8);
  for (int i = threadIdx.x; i < 512; i += 256) { int b = i >> 8, kk = i & 255, row = kg * 256 + kk, h = row >> 7, d = row & 127;
    const float* p = part + (size_t)((b * H + h) * NSPLIT) * 132;
    float M = -INFINITY; for (int s = 0; s < NSPLIT; ++s) M = fmaxf(M, p[s * 132]);
    float L = 0, a = 0; for (int s = 0; s < NSPLIT; ++s) { float wt = __builtin_amdgcn_exp2f(p[s * 132] - M); L += wt * p[s * 132 + 1]; a += wt * p[s * 132 + 4 + d]; }
    o[b][kk] = rb(a / L); }
  __syncthreads();
  float a[2][8] = {};
#pragma unroll
  for (int i = 0; i < 8; ++i) { float x0 = o[0][rl + 32 * i], x1 = o[1][rl + 32 * i];
#pragma unroll
    for (int k = 0; k < 4; ++k) { a[0][2 * k] += x0 * lo(w[i][k]); a[0][2 * k + 1] += x0 * hi(w[i][k]); a[1][2 * k] += x1 * lo(w[i][k]); a[1][2 * k + 1] += x1 * hi(w[i][k]); } }
#pragma unroll
  for (int k = 0; k < 8; ++k) { red[rl][0][cl * 8 + k] = a[0][k]; red[rl][1][cl * 8 + k] = a[1][k]; }
  __syncthreads();
  if (threadIdx.x < 128) { int b = threadIdx.x >> 6, c = threadIdx.x & 63; float s = 0; for (int j = 0; j < 32; ++j) s += red[j][b][c]; hp[(kg * 2 + b) * D + cg * 64 + c] = s; }
}

__global__ void __launch_bounds__(256) k_gu(const u16* x, const float* hp, const u16* wg_, const u16* wu, float* hbuf, float* g) {
  __shared__ float xin[2][D], xn[2][D], red[4][4][16];
  int cl = threadIdx.x & 1, rl = threadIdx.x >> 1;
  u32x4 wa[5], wb[5];
#pragma unroll
  for (int i = 0; i < 5; ++i) { wa[i] = ldnt(wg_ + (size_t)(rl + 128 * i) * F + blockIdx.x * 16 + cl * 8); wb[i] = ldnt(wu + (size_t)(rl + 128 * i) * F + blockIdx.x * 16 + cl * 8); }
  for (int i = threadIdx.x; i < 2 * D; i += 256) { int b = i / D, d = i % D; float s = 0;
#pragma unroll
    for (int k = 0; k < 16; ++k) s += hp[(k * 2 + b) * D + d];
    float h = rb(bf2f(x[i]) + rb(s)); xin[b][d] = h; if (blockIdx.x == 0) hbuf[i] = h; }
  __syncthreads();
  block_ln(xin[0], xn[0]); block_ln(xin[1], xn[1]);
  __syncthreads();
  float a[4][8] = {};
#pragma unroll
  for (int i = 0; i < 5; ++i) { float x0 = xn[0][rl + 128 * i], x1 = xn[1][rl + 128 * i];
#pragma unroll
    for (int k = 0; k < 4; ++k) { a[0][2 * k] += x0 * lo(wa[i][k]); a[0][2 * k + 1] += x0 * hi(wa[i][k]); a[1][2 * k] += x1 * lo(wa[i][k]); a[1][2 * k + 1] += x1 * hi(wa[i][k]);
      a[2][2 * k] += x0 * lo(wb[i][k]); a[2][2 * k + 1] += x0 * hi(wb[i][k]); a[3][2 * k] += x1 * lo(wb[i][k]); a[3][2 * k + 1] += x1 * hi(wb[i][k]); } }
  int w = threadIdx.x >> 6;
#pragma unroll
  for (int j = 0; j < 4; ++j)
#pragma unroll
    for (int k = 0; k < 8; ++k) { float v = a[j][k]; for (int o = 2; o < 64; o <<= 1) v += __shfl_xor(v, o); if ((threadIdx.x & 63) < 2) red[w][j][cl * 8 + k] = v; }
  __syncthreads();
  if (threadIdx.x < 32) { int b = threadIdx.x >> 4, c = threadIdx.x & 15;
    float gg = rb(red[0][b][c] + red[1][b][c] + red[2][b][c] + red[3][b][c]);
    float uu = rb(red[0][2 + b][c] + red[1][2 + b][c] + red[2][2 + b][c] + red[3][2 + b][c]);
    float t = 0.7978845608028654f * (gg + 0.044715f * gg * gg * gg);
    float ge = rb(0.5f * gg * (1.f + tanhf(t)));
    g[b * F + blockIdx.x * 16 + c] = rb(ge * uu); }
}

__global__ void __launch_bounds__(256) k_dn(const float* hbuf, const float* g, const u16* wd, u16* out) {
  __shared__ float gs[2][14 * 128], red[4][2][16];
  int cl = threadIdx.x & 1, rl = threadIdx.x >> 1;
  u32x4 w[14];
#pragma unroll
  for (int i = 0; i < 14; ++i) { int row = rl + 128 * i; row = row < F ? row : F - 1; w[i] = ldnt(wd + (size_t)row * D + blockIdx.x * 16 + cl * 8); }
  for (int i = threadIdx.x; i < 2 * 14 * 128; i += 256) { int b = i / (14 * 128), k = i % (14 * 128); gs[b][k] = k < F ? g[b * F + k] : 0.f; }
  __syncthreads();
  float a[2][8] = {};
#pragma unroll
  for (int i = 0; i < 14; ++i) { float x0 = gs[0][rl + 128 * i], x1 = gs[1][rl + 128 * i];
#pragma unroll
    for (int k = 0; k < 4; ++k) { a[0][2 * k] += x0 * lo(w[i][k]); a[0][2 * k + 1] += x0 * hi(w[i][k]); a[1][2 * k] += x1 * lo(w[i][k]); a[1][2 * k + 1] += x1 * hi(w[i][k]); } }
  int wv = threadIdx.x >> 6;
#pragma unroll
  for (int j = 0; j < 2; ++j)
#pragma unroll
    for (int k = 0; k < 8; ++k) { float v = a[j][k]; for (int o = 2; o < 64; o <<= 1) v += __shfl_xor(v, o); if ((threadIdx.x & 63) < 2) red[wv][j][cl * 8 + k] = v; }
  __syncthreads();
  if (threadIdx.x < 32) { int b = threadIdx.x >> 4, c = threadIdx.x & 15, col = blockIdx.x * 16 + c;
    float s = red[0][b][c] + red[1][b][c] + red[2][b][c] + red[3][b][c];
    out[b * D + col] = f2bf(hbuf[b * D + col] + rb(s)); }
}
__global__ void k_empty(int* p) { }

extern "C" void kernel_launch(void* const* d_in, const int* in_sizes, int n_in, void* d_out, int out_size, void* d_ws, size_t ws_size, hipStream_t stream) {
  const u16* x = (const u16*)d_in[0]; const u16* kc = (const u16*)d_in[1]; const u16* vc = (const u16*)d_in[2];
  const u16* wq = (const u16*)d_in[3]; const u16* wo = (const u16*)d_in[4];
  const u16* wg = (const u16*)d_in[6]; const u16* wu = (const u16*)d_in[7]; const u16* wd = (const u16*)d_in[8];
  char* ws = (char*)d_ws + (1 << 20);
  u16* q = (u16*)ws; float* part = (float*)(ws + 65536); float* hp = (float*)(ws + 65536 * 4); float* hbuf = (float*)(ws + 65536 * 6); float* g = (float*)(ws + 65536 * 7);
  if (WITH_MEMSET) hipMemsetAsync(d_ws, 0xff, WITH_MEMSET, stream);
  if (EXTRA_EMPTY) k_empty<<<1, 64, 0, stream>>>((int*)d_ws);
  k_q<<<64, 256, 0, stream>>>(x, wq, q);
  k_attn<<<256, 512, 0, stream>>>(q, kc, vc, part);
  k_wo<<<dim3(10, 16), 256, 0, stream>>>(part, wo, hp);
  k_gu<<<108, 256, 0, stream>>>(x, hp, wg, wu, hbuf, g);
  k_dn<<<40, 256, 0, stream>>>(hbuf, g, wd, (u16*)d_out);
}
```

```cpp
#include <hip/hip_runtime.h>
#include <stdint.h>

#define STAMPS 0
#define INF0 18
#define INFS 18
#define NSTART 5
#define INFE 18
#define DINF_WO 1
#define DINF_GU 1
#define DINF_DN 2
#define DRYRUN 1
#define NPRE 1
#define SLW1 4
#define SLW2 4
#define SLW3 4
#define SLP 2
#define BEAC 1
#define LSEX 32
#define DEL1 0
#define DEL2 0
#define DEL3 0

#define LAS __attribute__((address_space(3)))
typedef unsigned u32x4 __attribute__((ext_vector_type(4)));
typedef unsigned u32x2 __attribute__((ext_vector_type(2)));
typedef float f32x4 __attribute__((ext_vector_type(4)));
typedef float f32x2 __attribute__((ext_vector_type(2)));
typedef __bf16 bf16x2 __attribute__((ext_vector_type(2)));
typedef __bf16 bf16x8 __attribute__((ext_vector_type(8)));
typedef short s16x4 __attribute__((ext_vector_type(4)));
typedef unsigned short u16;
typedef unsigned long long u64;
typedef __amdgpu_buffer_rsrc_t rsrc_t;

constexpr int D = 640, NH = 32, SK = 8192, DH = 128, F = 1728, HD = NH * DH;
constexpr int NWG = 256, NTHR = 640;
constexpr int WS_EPOCH = 0, WS_FLAG = 4096, WS_HP = 8192, WS_G = 32768, WS_REC = 49152, WS_BC = 114688, WS_LSE = 147456, WS_QP = 180224, WS_END = WS_QP + 524288;
constexpr int NSLOT = 17, RING = NSLOT * 8192;
constexpr int L_SCR = RING;
constexpr int L_H = L_SCR + 16384;
constexpr int L_RED = L_H + 3840;
constexpr int L_Q = L_RED + 2048;
constexpr int L_CTL = L_Q + 320;
constexpr int L_CNT = L_CTL + 128;
constexpr int L_END = L_CNT + 256;
static_assert(L_END <= 163840, "LDS");
constexpr unsigned SENT = 0xffffffffu;
constexpr int OOB = 0x7fffffff;
constexpr float L2E = 1.4426950408889634f;
constexpr int SPIN_CAP = 4000000;
constexpr int AUX_NT = 2, AUX_SC1 = 16;

__device__ __forceinline__ float rb(float f) { return (float)(__bf16)f; }
__device__ __forceinline__ float lo(unsigned v) { return __uint_as_float(v << 16); }
__device__ __forceinline__ float hi(unsigned v) { return __uint_as_float(v & 0xffff0000u); }
__device__ __forceinline__ float asf(unsigned v) { return __uint_as_float(v); }
__device__ __forceinline__ unsigned asu(float v) { return __float_as_uint(v); }
__device__ __forceinline__ unsigned pk(float a, float b) { bf16x2 r = {(__bf16)a, (__bf16)b}; return __builtin_bit_cast(unsigned, r); }
__device__ __forceinline__ unsigned umin(unsigned a, unsigned b) { return a < b ? a : b; }
__device__ __forceinline__ unsigned umax(unsigned a, unsigned b) { return a > b ? a : b; }
__device__ __forceinline__ unsigned nosent(unsigned v) { return umin(v, SENT - 1); }
__device__ __forceinline__ unsigned mx4(u32x4 v) { return umax(umax(v[0], v[1]), umax(v[2], v[3])); }
__device__ __forceinline__ float dot2(unsigned a, unsigned b, float c) { return __builtin_amdgcn_fdot2_f32_bf16(__builtin_bit_cast(bf16x2, a), __builtin_bit_cast(bf16x2, b), c, false); }
template <int CTRL> __device__ __forceinline__ float dpp(float x) { return __builtin_bit_cast(float, __builtin_amdgcn_mov_dpp(__builtin_bit_cast(int, x), CTRL, 0xf, 0xf, true)); }
template <int CTRL> __device__ __forceinline__ unsigned dppu(unsigned x) { return (unsigned)__builtin_amdgcn_mov_dpp((int)x, CTRL, 0xf, 0xf, true); }
constexpr int X1 = 0xB1, X2 = 0x4E, R8 = 0x128, R4 = 0x124, R2 = 0x122, R1 = 0x121;
__device__ __forceinline__ float row_sum16(float x) { x += dpp<R8>(x); x += dpp<R4>(x); x += dpp<R2>(x); x += dpp<R1>(x); return x; }
__device__ __forceinline__ float sw16_add(float a, float b) { auto r = __builtin_amdgcn_permlane16_swap(asu(a), asu(b), false, false); return asf(r[0]) + asf(r[1]); }
__device__ __forceinline__ float sw32_add(float a, float b) { auto r = __builtin_amdgcn_permlane32_swap(asu(a), asu(b), false, false); return asf(r[0]) + asf(r[1]); }
__device__ __forceinline__ float sw16_max(float a) { auto r = __builtin_amdgcn_permlane16_swap(asu(a), asu(a), false, false); return fmaxf(asf(r[0]), asf(r[1])); }
__device__ __forceinline__ float sw32_max(float a) { auto r = __builtin_amdgcn_permlane32_swap(asu(a), asu(a), false, false); return fmaxf(asf(r[0]), asf(r[1])); }
__device__ __forceinline__ float wave_sum(float x) { x = row_sum16(x); x = sw16_add(x, x); return sw32_add(x, x); }
__device__ __forceinline__ float ex2(float x) { return __builtin_amdgcn_exp2f(x); }
__device__ __forceinline__ float rcp(float x) { return __builtin_amdgcn_rcpf(x); }
__device__ __forceinline__ rsrc_t mk(const void* p, int n) { return __builtin_amdgcn_make_buffer_rsrc((void*)p, 0, n, 0x00020000); }
__device__ __forceinline__ f32x4 mfma(u32x4 a, u32x4 b, f32x4 c) { return __builtin_amdgcn_mfma_f32_16x16x32_bf16(__builtin_bit_cast(bf16x8, a), __builtin_bit_cast(bf16x8, b), c, 0, 0, 0); }
__device__ __forceinline__ u32x4 ld16(rsrc_t r, int o) { return __builtin_amdgcn_raw_buffer_load_b128(r, o, 0, AUX_SC1); }
__device__ __forceinline__ unsigned ld4(rsrc_t r, int o) { return __builtin_amdgcn_raw_buffer_load_b32(r, o, 0, AUX_SC1); }
__device__ __forceinline__ void st16(u32x4 v, rsrc_t r, int o) { __builtin_amdgcn_raw_buffer_store_b128(v, r, o, 0, AUX_SC1); }
__device__ __forceinline__ void st4(unsigned v, rsrc_t r, int o) { __builtin_amdgcn_raw_buffer_store_b32(v, r, o, 0, AUX_SC1); }
template <int N> __device__ __forceinline__ void pause() { if (N > 0) { for (int i = 0; i < N / 16; ++i) __builtin_amdgcn_s_sleep(16); if (N % 16) __builtin_amdgcn_s_sleep(N % 16); } }
#define LGKM0() asm volatile("s_waitcnt lgkmcnt(0)" ::: "memory")
#define FENCE() asm volatile("" ::: "memory")
#define VM0() asm volatile("s_waitcnt vmcnt(0)" ::: "memory")
#define BAR() do { LGKM0(); __builtin_amdgcn_s_barrier(); FENCE(); } while (0)
#define PIN(x) asm volatile("" : "+v"(x))
#define STAMP(i) do { } while (0)
#define STAMP_OUT() do { } while (0)

constexpr int WMAX = INF0 + 1, JP = WMAX & 3;
static_assert(INFS - 1 <= WMAX && INFE - 1 <= WMAX, "the published count assumes the longest queue");
#define LD1(j, base, off) "s_waitcnt vmcnt(%c[W])\n\t.if %c[P] == " #j "\n\tds_add_u32 %[ca], %[four]\n\t.endif\n\tglobal_load_lds_dwordx4 %[vo], " base " offset:" #off " nt\n\t"
#define LD4(base) LD1(0, base, 0) LD1(1, base, 1024) LD1(2, base, 2048) LD1(3, base, 3072)
#define ADV "v_add_u32 %[vo], 0x1000, %[vo]\n\ts_add_u32 %[lds], %[lds], 0x2000\n\tv_and_b32 %[vo], 0x7ffff, %[vo]\n\ts_cmp_eq_u32 %[lds], %[ring]\n\ts_cselect_b32 %[lds], %[zero], %[lds]\n\t"
template <int W> __device__ __forceinline__ void ld_slots(int n, const char* kb, const char* vb, unsigned& voff, unsigned& lds, unsigned base, unsigned ca, unsigned four) {
  lds = __builtin_amdgcn_readfirstlane(lds); n = __builtin_amdgcn_readfirstlane(n);
  unsigned keep;
  asm volatile("s_mov_b32 %[keep], m0\n1:\n\ts_mov_b32 m0, %[lds]\n\ts_sub_u32 %[n], %[n], 1\n\t" LD4("%[kb]") "s_add_u32 m0, m0, 0x1000\n\t" LD4("%[vb]") ADV "s_cmp_lg_u32 %[n], 0\n\ts_cbranch_scc1 1b\n\ts_mov_b32 m0, %[keep]"
     : [keep] "=&s"(keep), [n] "+s"(n), [vo] "+v"(voff), [lds] "+s"(lds) : [kb] "s"(kb), [vb] "s"(vb), [ca] "v"(ca), [four] "v"(four), [ring] "s"(base + RING), [zero] "s"(base), [W] "i"(W), [P] "i"(JP) : "memory", "scc");
}
template <int W> __device__ __forceinline__ void ld_halves(int n, const char* kb, unsigned& voff, unsigned& lds, unsigned base, unsigned ca, unsigned four) {
  lds = __builtin_amdgcn_readfirstlane(lds); n = __builtin_amdgcn_readfirstlane(n);
  unsigned keep;
  asm volatile("s_mov_b32 %[keep], m0\n1:\n\ts_mov_b32 m0, %[lds]\n\ts_sub_u32 %[n], %[n], 1\n\t" LD4("%[kb]") ADV "s_cmp_lg_u32 %[n], 0\n\ts_cbranch_scc1 1b\n\ts_mov_b32 m0, %[keep]"
     : [keep] "=&s"(keep), [n] "+s"(n), [vo] "+v"(voff), [lds] "+s"(lds) : [kb] "s"(kb), [ca] "v"(ca), [four] "v"(four), [ring] "s"(base + RING), [zero] "s"(base), [W] "i"(W), [P] "i"(JP) : "memory", "scc");
}
template <int W> __device__ __forceinline__ void ld_singles(const char* kb, unsigned voff, unsigned lds, unsigned base, unsigned ca, int& cnt) {
  int n = 8; lds = __builtin_amdgcn_readfirstlane(lds);
  unsigned keep;
  asm volatile("s_mov_b32 %[keep], m0\n1:\n\ts_mov_b32 m0, %[lds]\n\ts_sub_u32 %[n], %[n], 1\n\ts_waitcnt vmcnt(%c[W])\n\tds_write_b32 %[ca], %[cnt]\n\tglobal_load_lds_dwordx4 %[vo], %[kb] nt\n\tv_add_u32 %[cnt], 1, %[cnt]\n\t"
     ADV "s_cmp_lg_u32 %[n], 0\n\ts_cbranch_scc1 1b\n\ts_mov_b32 m0, %[keep]"
     : [keep] "=&s"(keep), [n] "+s"(n), [vo] "+v"(voff), [lds] "+s"(lds), [cnt] "+v"(cnt) : [kb] "s"(kb), [ca] "v"(ca), [ring] "s"(base + RING), [zero] "s"(base), [W] "i"(W) : "memory", "scc");
}

__global__ void __launch_bounds__(NTHR) k_block(const u16* __restrict__ x, const char* __restrict__ K, const char* __restrict__ V, const u16* __restrict__ wq,
                                                const u16* __restrict__ wo, const u16* __restrict__ wg, const u16* __restrict__ wu, const u16* __restrict__ wd,
                                                u16* __restrict__ out, char* __restrict__ ws) {
  extern __shared__ __attribute__((aligned(16))) char lds_raw[];
  LAS char* L = (LAS char*)lds_raw;
  volatile LAS int* ctl = (volatile LAS int*)(L + L_CTL);
  const int lane = threadIdx.x & 63, w = __builtin_amdgcn_readfirstlane(threadIdx.x >> 6);
  const int g = blockIdx.x;
  {
    const int t = threadIdx.x;
    if (t < 512) *(LAS u32x4*)(L + L_SCR + (t >> 6) * 2048 + (t & 63) * 16) = u32x4{SENT, SENT, SENT, SENT};
    else if (t < 592) *(LAS u32x4*)(L + L_H + 2560 + (t - 512) * 16) = u32x4{0x3f803f80u, 0x3f803f80u, 0x3f803f80u, 0x3f803f80u};
    else if (t < 612) *(LAS u32x4*)(L + L_Q + (t - 592) * 16) = u32x4{SENT, SENT, SENT, SENT};
    else if (t < 620) *(LAS u32x4*)(L + L_CTL + (t - 612) * 16) = u32x4{0, 0, 0, 0};
    else if (t < 636) { const unsigned c0 = (unsigned)(JP - WMAX - 4); *(LAS u32x4*)(L + L_CNT + (t - 620) * 16) = u32x4{c0, c0, c0, c0}; }
  }
  asm volatile("" :: "s"(x), "s"(K), "s"(V), "s"(wq), "s"(wo), "s"(wg), "s"(wu), "s"(wd), "s"(out), "s"(ws));
  BAR();
  const int xs = g & 7, a = g >> 3;
  const bool r_wo = a < 10;
  const int wo_t = xs * 10 + a, wo_kg = wo_t / 20, wo_cg = wo_t % 20;
  int gu_quad = -1; if (a >= 10 && a < 22) gu_quad = xs + 8 * ((a - 10) >> 2); else if (a >= 22 && a < 26 && xs < 3) gu_quad = xs + 24;
  const bool r_gu = gu_quad >= 0; const int gu_cg = gu_quad * 4 + ((a - 10) & 3);
  int dn_quad = -1; if (a >= 26 && a < 30) dn_quad = xs; else if (a >= 22 && a < 26 && xs >= 6) dn_quad = xs + 2;
  const bool r_dn = dn_quad >= 0; const int dn_cg = dn_quad * 4 + ((a - 10) & 3);

  if (w == 0) {
    STAMP(1);
    __builtin_amdgcn_s_setprio(3);
    const unsigned lb = (unsigned)(size_t)L, ca = lb + L_CNT + lane * 4, four = 4;
    const unsigned rot = (g * 2654435761u) >> 28;
    const char* kb = K + (size_t)g * 524288; const char* vb = V + (size_t)g * 524288;
    unsigned voff = lane * 16 + rot * 4096, lo_ = lb;
    const int dinf = r_wo ? DINF_WO : r_gu ? DINF_GU : r_dn ? DINF_DN : 0;
    unsigned vo14 = 0, lo14 = 0;
#pragma unroll 1
    for (int s = 0; s < 128;) {
      const int r = s >> 3;
      int n = 8;
      if (r >= 2) {
        bool all = false;
        if ((s & 7) == 0) {
          FENCE();
          const u32x4 f0 = *(const LAS u32x4*)(L + L_CTL + 32), f1 = *(const LAS u32x4*)(L + L_CTL + 48);
          const unsigned mn = umin(umin(umin(f0[0], f0[1]), umin(f0[2], f0[3])), umin(umin(f1[0], f1[1]), f1[2]));
          all = (int)__builtin_amdgcn_readfirstlane(umin(mn, f1[3] + 1)) >= r - 1;
        }
        if (!all) {
          n = 1;
          const int need = s >= NSLOT ? (s - NSLOT) / 8 + 1 : 0;
          int sp = 0; while (__builtin_amdgcn_readfirstlane(ctl[8 + ((s - NSLOT) & 7)]) < need && ++sp < SPIN_CAP) __builtin_amdgcn_s_sleep(1);
        }
      }
      const int d = (r >= 2 && r < 11) ? dinf : 0;
      if (s == 112) { vo14 = voff; lo14 = lo_; }
      if (r >= 14) ld_halves<INFE - 1>(n, kb, voff, lo_, lb, ca, four);
      else if (INFS != INF0 && r == 0) ld_slots<INFS - 1>(n, kb, vb, voff, lo_, lb, ca, four);
      else if (d == 0) ld_slots<INF0 - 1>(n, kb, vb, voff, lo_, lb, ca, four);
      else if (d == 1) ld_slots<INF0>(n, kb, vb, voff, lo_, lb, ca, four);
      else ld_slots<INF0 + 1>(n, kb, vb, voff, lo_, lb, ca, four);
      s += n;
    }
    lo14 += 4096;
    ld_halves<INFE - 1>(8, vb, vo14, lo14, lb + 4096, ca, four);
    int cnt = 992 - (INFE - 1);
#pragma unroll 1
    for (int t = 0; t < 4; ++t) ld_singles<INFE - 1>(vb + t * 1024, vo14, lo14 + t * 1024, lb + 4096 + t * 1024, ca, cnt);
    STAMP(2);
#define DR(n) if (INFE - 1 >= n) { asm volatile("s_waitcnt vmcnt(" #n ")" ::: "memory"); *(volatile LAS int*)(L + L_CNT) = 1024 - n; }
    DR(36) DR(32) DR(28) DR(24) DR(20) DR(18) DR(16) DR(14) DR(12) DR(10) DR(8) DR(7) DR(6) DR(5) DR(4) DR(3) DR(2) DR(1) DR(0)
#undef DR
    STAMP(3);
    STAMP_OUT();
    return;
  }
  const bool dry = (w == 1);
  const int cw = dry ? 0 : w - 2;
  const int bh = g >> 2, sp4 = g & 3, b = bh >> 5, h = bh & 31;
  const int col = lane & 15, kq = lane >> 4;
  LAS char* scr = L + L_SCR + cw * 2048;

  u32x4 FB[8];
#pragma unroll
  for (int i = 0; i < 8; ++i) FB[i] = u32x4{0, 0, 0, 0};
  unsigned tagA, tagB;
  const rsrc_t rws = mk(ws, WS_END);
  u32x4 qv = {0, 0, 0, 0}, kk[4], vv[4];
#pragma unroll
  for (int t = 0; t < 4; ++t) { kk[t] = qv; vv[t] = qv; }

  if (dry) {
    STAMP(4);
    const rsrc_t rx = mk(x, 2 * D * 2);
    u32x4 xa[2]; unsigned xb[2];
#pragma unroll
    for (int bb = 0; bb < 2; ++bb) { xa[bb] = __builtin_amdgcn_raw_buffer_load_b128(rx, bb * 1280 + lane * 16, 0, 0); xb[bb] = __builtin_amdgcn_raw_buffer_load_b32(rx, bb * 1280 + 1024 + lane * 4, 0, 0); }
    const u32x2 e = __builtin_amdgcn_raw_buffer_load_b64(rws, WS_EPOCH, 0, AUX_SC1);
    float mb[2], den[2];
#pragma unroll
    for (int bb = 0; bb < 2; ++bb) {
      *(LAS u32x4*)(L + L_H + bb * 1280 + lane * 16) = xa[bb]; *(LAS unsigned*)(L + L_H + bb * 1280 + 1024 + lane * 4) = xb[bb];
      float v[10];
#pragma unroll
      for (int k = 0; k < 4; ++k) { v[2 * k] = lo(xa[bb][k]); v[2 * k + 1] = hi(xa[bb][k]); }
      v[8] = lo(xb[bb]); v[9] = hi(xb[bb]);
      float s = ((v[0] + v[1]) + (v[2] + v[3])) + ((v[4] + v[5]) + (v[6] + v[7])) + (v[8] + v[9]);
      const float mean = wave_sum(s) * (1.f / D);
      float q0 = 0.f, q1 = 0.f;
#pragma unroll
      for (int k = 0; k < 5; ++k) { const float d0 = v[2 * k] - mean, d1 = v[2 * k + 1] - mean; q0 = fmaf(d0, d0, q0); q1 = fmaf(d1, d1, q1); }
      const float var = wave_sum(q0 + q1) * (1.f / D);
      mb[bb] = rb(mean); den[bb] = rb(sqrtf(rb(rb(var) + 1e-5f)));
    }
    tagA = e[0] + 1; tagB = e[1] + 1;
    if (lane == 0) { *(LAS f32x4*)(L + L_CTL + 64) = f32x4{mb[0], den[0], mb[1], den[1]}; *(LAS u32x2*)(L + L_CTL + 80) = u32x2{tagA, tagB}; FENCE(); ctl[1] = 1; }
    STAMP(24);
    int o = OOB;
    if (lane < 16) o = WS_REC + g * 256 + lane * 16;
    else if (lane < 32) { if (r_wo) o = WS_HP + ((wo_kg * 2 + ((lane >> 3) & 1)) * D + wo_cg * 32 + (lane & 7) * 4) * 4; }
    else if (lane < 36) { if (r_gu) o = WS_G + gu_cg * 128 + (lane & 3) * 16; }
    st16(u32x4{SENT, SENT, SENT, SENT}, rws, o);
    st4(SENT, rws, lane == 0 ? WS_LSE + g * 128 : lane == 1 ? WS_BC + g * 128 : OOB);
    VM0();
    __builtin_amdgcn_raw_buffer_store_b64(u32x2{tagA, tagB}, rws, lane == 0 ? WS_FLAG + g * 8 : OOB, 0, AUX_SC1);
    STAMP(5);
  } else {
    if (cw == 0) STAMP(8);
    const int j = b * 4 + sp4, row0 = 80 * j + 10 * cw;
    const rsrc_t rq = mk(wq + (size_t)row0 * HD + h * DH, 9 * HD * 2 + 256);
    u32x4 wv[3];
#pragma unroll
    for (int i = 0; i < 3; ++i) wv[i] = __builtin_amdgcn_raw_buffer_load_b128(rq, (4 * i + kq) * (HD * 2) + col * 16, 0, AUX_NT);
    { int sp = 0; while (ctl[1] == 0 && ++sp < SPIN_CAP) __builtin_amdgcn_s_sleep(0); }
    FENCE();
    {
      const f32x4 st = *(LAS f32x4*)(L + L_CTL + 64); const u32x2 tg = *(LAS u32x2*)(L + L_CTL + 80);
      tagA = tg[0]; tagB = tg[1];
      float acc[2][8];
#pragma unroll
      for (int bb = 0; bb < 2; ++bb) {
#pragma unroll
        for (int k = 0; k < 8; ++k) acc[bb][k] = 0.f;
#pragma unroll
        for (int i = 0; i < 3; ++i) { const float xn = rb(rb(lo(*(LAS u16*)(L + L_H + bb * 1280 + (row0 + 4 * i + kq) * 2)) - st[2 * bb]) / st[2 * bb + 1]);
#pragma unroll
          for (int k = 0; k < 4; ++k) { acc[bb][2 * k] = fmaf(xn, lo(wv[i][k]), acc[bb][2 * k]); acc[bb][2 * k + 1] = fmaf(xn, hi(wv[i][k]), acc[bb][2 * k + 1]); } }
      }
      float u[8]; u32x4 t;
#pragma unroll
      for (int k = 0; k < 8; ++k) u[k] = sw32_add(acc[0][k], acc[1][k]);
#pragma unroll
      for (int k = 0; k < 4; ++k) t[k] = nosent(asu(sw16_add(u[k], u[k + 4])));
      *(LAS u32x4*)(scr + ((lane >> 5) * 128 + col * 8 + (kq & 1) * 4) * 4) = t;
    }
    if (cw == 0) STAMP(9);
    {
      const LAS char* p = L + L_SCR + (kq * 2) * 2048 + (((lane >> 3) & 1) * 128 + 16 * cw + (lane & 7) * 2) * 4;
      u32x2 t0, t1; int sp = 0;
      while (true) {
        FENCE();
        t0 = *(const LAS u32x2*)p; t1 = *(const LAS u32x2*)(p + 2048);
        if (__all(umax(umax(t0[0], t0[1]), umax(t1[0], t1[1])) != SENT) || ++sp > SPIN_CAP) break;
      }
      float r0 = asf(t0[0]) + asf(t1[0]), r1 = asf(t0[1]) + asf(t1[1]);
      r0 = sw16_add(r0, r0); r1 = sw16_add(r1, r1); r0 = sw32_add(r0, r0); r1 = sw32_add(r1, r1);
      st16(u32x4{asu(r0), tagA, asu(r1), tagB}, rws, lane < 16 ? WS_QP + (((h * 2 + (lane >> 3)) * 8 + j) * 64 + 8 * cw + (lane & 7)) * 16 : OOB);
    }
    if (cw == 0) STAMP(10);
    {
      int sp = 0; while (ctl[32] < cw * 8 + 8 && ++sp < SPIN_CAP) __builtin_amdgcn_s_sleep(1);
      FENCE();
      const LAS u32x4* sl = (const LAS u32x4*)(L + cw * 8192) + lane;
#pragma unroll
      for (int t = 0; t < 4; ++t) { kk[t] = sl[t * 64]; vv[t] = sl[256 + t * 64]; }
      LGKM0(); ctl[8 + cw] = 1;
    }
    if (NPRE >= 2) {
      int sp = 0; while (ctl[32] < cw * 8 + 72 && ++sp < SPIN_CAP) __builtin_amdgcn_s_sleep(1);
      FENCE();
      const LAS u32x4* sl = (const LAS u32x4*)(L + (cw + 8) * 8192) + lane;
#pragma unroll
      for (int t = 0; t < 4; ++t) { FB[t] = sl[t * 64]; FB[4 + t] = sl[256 + t * 64]; }
      LGKM0(); ctl[8 + cw] = 2;
    }
    {
      const int o = WS_QP + (((h * 2 + b) * 8 + (lane >> 3)) * 64 + 8 * cw + (lane & 7)) * 16;
      u32x4 v; int sp = 0;
      while (true) {
        FENCE();
        v = ld16(rws, o);
        if (__all(v[1] == tagA && v[3] == tagB) || ++sp > SPIN_CAP) break;
      }
      float q0 = asf(v[0]), q1 = asf(v[2]);
      q0 += dpp<R8>(q0); q1 += dpp<R8>(q1);
      q0 = sw16_add(q0, q0); q1 = sw16_add(q1, q1); q0 = sw32_add(q0, q0); q1 = sw32_add(q1, q1);
      if (lane < 8) *(volatile LAS unsigned*)(L + L_Q + (8 * cw + lane) * 4) = nosent(pk(q0, q1));
    }
    if (cw == 0) STAMP(11);
    { int sp = 0;
      while (true) { FENCE(); qv = *(const LAS u32x4*)(L + L_Q + col * 16); if (__all(mx4(qv) != SENT) || ++sp > SPIN_CAP) break; } }
    if (cw == 0) STAMP(12);
  }

  const rsrc_t rt = mk(ws, dry ? 0 : WS_END);
  int a_rec = kq == 0 ? WS_REC + g * 256 + col * 16 : OOB;
  int a_lse = lane == 0 ? WS_LSE + g * 128 : OOB;
  int a_bc = lane == 0 ? WS_BC + g * 128 : OOB;
  int a_w, a_p, a_s, a_x;
  if (r_wo) { const int hh = wo_kg * 8 + cw, rec = col & 7;
    const int gp = ((rec >> 2) * 32 + hh) * 4 + (rec & 3);
    a_w = WS_LSE + gp * 128; a_p = WS_REC + gp * 256 + kq * 16;
    a_s = lane < 16 ? WS_HP + ((wo_kg * 2 + (lane >> 3)) * D + wo_cg * 32 + (lane & 7) * 4) * 4 : OOB; a_x = 0;
  } else if (r_gu) {
    { const int t = (lane & 3) * 20 + g % 20; a_w = WS_BC + ((t % 10) * 8 + t / 10) * 128; }
    a_p = WS_HP + (cw * 64 + lane) * 16;
    a_s = (lane < 32 && (lane & 7) == 0) ? WS_G + gu_cg * 128 + lane * 2 : OOB; a_x = 0;
  } else {
    a_w = WS_BC + (g - 96) * 128;
    a_p = WS_G + (cw * 14 + (kq >> 1)) * 128 + (col & 1) * 32 + (kq & 1) * 16;
    a_s = (lane < 32 && (lane & 1) == 0) ? ((kq & 1) * D + dn_cg * 16 + col) * 2 : OOB; a_x = (kq & 1) * D + dn_cg * 16 + col;
  }
  if (dry) a_x = 0;
  int a_p2 = cw == 7 ? OOB - 2048 : a_p;
  unsigned l_red = (unsigned)(size_t)(L + L_RED), l_scr = (unsigned)(size_t)scr + lane * 8;
  unsigned l_w = l_red + 4 * (r_wo ? cw * 64 + (col >> 2) * 32 + (col & 1) * 16 + kq * 4 : cw * 64 + col * 16 + kq * 4);
  unsigned l_r = l_red + 4 * (r_wo ? kq * 128 + col * 4 : r_gu ? (lane >> 5) * 64 + col : (lane >> 5) * 256 + (lane & 31));
  unsigned l_h = (unsigned)(size_t)(L + L_H) + (col < 2 ? col : 2) * 1280 + ((cw >> 1) * 160 + kq * 8) * 2;
  unsigned l_hw = (unsigned)(size_t)(L + L_H) + (cw * 64 + lane) * 8;
  PIN(a_rec); PIN(a_lse); PIN(a_bc); PIN(a_w); PIN(a_p); PIN(a_p2); PIN(a_s); PIN(a_x); PIN(l_scr); PIN(l_w); PIN(l_r); PIN(l_h); PIN(l_hw);
#define LP(T, a) ((LAS T*)(size_t)(a))

  const u16* wp = r_wo ? wo + (size_t)(wo_kg * 1024 + cw * 128) * D + wo_cg * 32 : r_gu ? ((cw & 1) ? wu : wg) + (size_t)((cw >> 1) * 160) * F + gu_cg * 16 : wd + (size_t)(cw * 224) * D + dn_cg * 16;
  const int wstr = r_gu ? F * 2 : D * 2;
  const rsrc_t rw = mk(wp, r_wo ? 127 * D * 2 + 64 : r_gu ? 159 * F * 2 + 32 : r_dn ? (cw == 7 ? 159 : 223) * D * 2 + 32 : 0);
  int woff = r_wo ? (lane >> 2) * wstr + (lane & 3) * 16 : (lane >> 1) * wstr + (lane & 1) * 16;
  const int wstep = (r_wo ? 16 : 32) * wstr;
  const int trow = r_wo ? 64 : 32, ta0 = (8 * kq + ((lane >> 2) & 3)) * trow + (lane & 3) * 8, ta1 = ta0 + (r_wo ? 32 : 1024);
  float m = -INFINITY, l = 0.f, o8[8], inv = 0.f;
#pragma unroll
  for (int k = 0; k < 8; ++k) o8[k] = 0.f;
  unsigned off;
  const bool fin = cw == 7 || dry;
#define WAIT_LANDED(thr, SL) do { if (have < (thr)) { int sp_ = 0; while ((have = __builtin_amdgcn_readfirstlane(ctl[32])) < (thr) && ++sp_ < SPIN_CAP) __builtin_amdgcn_s_sleep(SL); } FENCE(); } while (0)
  int have = dry ? OOB : -OOB;
  float p[4] = {0.f, 0.f, 0.f, 0.f}, pn[4] = {0.f, 0.f, 0.f, 0.f};
  unsigned lse = 0;
#define PV(t) _Pragma("unroll") for (int k = 0; k < 4; ++k) { o8[2 * k] = fmaf(p[t], lo(vv[t][k]), o8[2 * k]); o8[2 * k + 1] = fmaf(p[t], hi(vv[t][k]), o8[2 * k + 1]); }
#define PINO() asm volatile("" : "+v"(o8[0]), "+v"(o8[1]), "+v"(o8[2]), "+v"(o8[3]), "+v"(o8[4]), "+v"(o8[5]), "+v"(o8[6]), "+v"(o8[7]) :: "memory")
#define SCORES() float sc[4]; _Pragma("unroll") for (int t = 0; t < 4; ++t) { float d = 0.f; _Pragma("unroll") for (int k = 0; k < 4; ++k) d = dot2(kk[t][k], qv[k], d); \
      d = row_sum16(d); sc[t] = rb(rb(d) * 0.08838834764831845f); } \
    float mn = fmaxf(fmaxf(m, fmaxf(sc[0], sc[1])), fmaxf(sc[2], sc[3]));
  if (!dry) {
#pragma unroll 1
    for (int n = 0; n < NPRE; ++n) {
      SCORES()
      const float al = ex2((m - mn) * L2E), mneg = -mn * L2E; m = mn;
#pragma unroll
      for (int t = 0; t < 4; ++t) p[t] = ex2(fmaf(sc[t], L2E, mneg));
      l = fmaf(l, al, (p[0] + p[1]) + (p[2] + p[3]));
#pragma unroll
      for (int k = 0; k < 8; ++k) o8[k] *= al;
      PV(0) PV(1) PV(2) PV(3)
#pragma unroll
      for (int t = 0; t < 4; ++t) { kk[t] = FB[t]; vv[t] = FB[4 + t]; }
    }
  }
  off = ((NPRE * 8 + cw) % NSLOT) * 8192;
#pragma unroll 1
  for (int n = (dry && DRYRUN) ? 15 : dry ? 18 : NPRE; n < 18; ++n) {
    if (n == 16) off = ((112 + cw) % NSLOT) * 8192;
    const LAS u32x4* sl = (const LAS u32x4*)(L + off) + lane;
    off += 8 * 8192; if (off >= RING) off -= RING;
    const int tk = n < 14 ? (n * 8 + cw) * 8 + 4 : 452 + n * 32 + 4 * cw;
    const int tv = n < 14 ? tk + 1 : n == 16 ? 961 + 4 * cw : 993 + cw, tvs = n == 17 ? 8 : 1;
    if (n < 16) {
      WAIT_LANDED(tk, 1);
#pragma unroll
      for (int t = 0; t < 4; ++t) kk[t] = sl[t * 64];
      SCORES()
      if (n == 15) {
        mn = sw32_max(sw16_max(mn));
        if (!dry) *(volatile LAS unsigned*)(L + L_Q + 256 + cw * 4) = nosent(asu(mn));
        u32x4 t0, t1; int sp = 0;
        while (true) { FENCE(); t0 = *(const LAS u32x4*)(L + L_Q + 256); t1 = *(const LAS u32x4*)(L + L_Q + 272); if (__all(umax(mx4(t0), mx4(t1)) != SENT) || dry || ++sp > SPIN_CAP) break; __builtin_amdgcn_s_sleep(1); }
        mn = fmaxf(fmaxf(fmaxf(asf(t0[0]), asf(t0[1])), fmaxf(asf(t0[2]), asf(t0[3]))), fmaxf(fmaxf(asf(t1[0]), asf(t1[1])), fmaxf(asf(t1[2]), asf(t1[3]))));
      }
      const float al = ex2((m - mn) * L2E), mneg = -mn * L2E; m = mn;
#pragma unroll
      for (int t = 0; t < 4; ++t) { pn[t] = ex2(fmaf(sc[t], L2E, mneg)); p[t] = n == 15 ? p[t] * al : pn[t]; }
      l = fmaf(l, al, (pn[0] + pn[1]) + (pn[2] + pn[3]));
#pragma unroll
      for (int k = 0; k < 8; ++k) o8[k] *= al;
      if (n == 15) {
        float ll = sw16_add(l, l); ll = sw32_add(ll, ll);
        if (!dry) *(volatile LAS unsigned*)(L + L_Q + 288 + cw * 4) = nosent(asu(ll));
        { int sp = 0; while (ctl[2] == 0 && !dry && ++sp < SPIN_CAP) __builtin_amdgcn_s_sleep(1); }
        if (fin) {
          u32x4 t0, t1; int sp = 0;
          while (true) { FENCE(); t0 = *(const LAS u32x4*)(L + L_Q + 288); t1 = *(const LAS u32x4*)(L + L_Q + 304); if (__all(umax(mx4(t0), mx4(t1)) != SENT) || dry || ++sp > SPIN_CAP) break; }
          const float Ls = ((asf(t0[0]) + asf(t0[1])) + (asf(t0[2]) + asf(t0[3]))) + ((asf(t1[0]) + asf(t1[1])) + (asf(t1[2]) + asf(t1[3])));
          inv = rcp(Ls);
          lse = nosent(asu(fmaf(mn, L2E, __builtin_amdgcn_logf(Ls))));
          STAMP(27);
        }
      }
      asm volatile("" : "+v"(p[0]), "+v"(p[1]), "+v"(p[2]), "+v"(p[3]), "+v"(pn[0]), "+v"(pn[1]), "+v"(pn[2]), "+v"(pn[3]), "+v"(l), "+v"(inv), "+v"(lse) :: "memory");
      if (n >= 14) continue;
    }
    if (n == 17 && fin) { WAIT_LANDED(1024 - LSEX, 0); st4(lse, rt, a_lse); STAMP(6); }
    WAIT_LANDED(tv, 1); vv[0] = sl[256];
    WAIT_LANDED(tv + tvs, 0); vv[1] = sl[320];
    PV(0)
    PINO();
    WAIT_LANDED(tv + 2 * tvs, 0); vv[2] = sl[384];
    PV(1) PV(2)
    PINO();
    WAIT_LANDED(tv + 3 * tvs, 0); vv[3] = sl[448];
    LGKM0(); if (!dry) ctl[8 + cw] = n + 1; PIN(vv[3]);
    PV(3)
#pragma unroll
    for (int t = 0; t < 4; ++t) p[t] = pn[t];
    if (n >= 2 && n < 10) {
      const u32x4 t = __builtin_amdgcn_raw_buffer_load_b128(rw, woff, 0, AUX_NT);
      woff += wstep;
      *(LAS u32x4*)(scr + (n & 1) * 1024 + lane * 16) = t;
      if (n & 1) {
        LGKM0();
        const u32x2 a0 = __builtin_bit_cast(u32x2, __builtin_amdgcn_ds_read_tr16_b64_v4i16((LAS s16x4*)(scr + ta0))), a1 = __builtin_bit_cast(u32x2, __builtin_amdgcn_ds_read_tr16_b64_v4i16((LAS s16x4*)(scr + ta0 + 4 * trow)));
        const u32x2 c0 = __builtin_bit_cast(u32x2, __builtin_amdgcn_ds_read_tr16_b64_v4i16((LAS s16x4*)(scr + ta1))), c1 = __builtin_bit_cast(u32x2, __builtin_amdgcn_ds_read_tr16_b64_v4i16((LAS s16x4*)(scr + ta1 + 4 * trow)));
#pragma unroll
        for (int i = 0; i < 6; ++i) FB[i] = FB[i + 2];
        FB[6] = u32x4{a0[0], a0[1], a1[0], a1[1]}; FB[7] = u32x4{c0[0], c0[1], c1[0], c1[1]};
        LGKM0();
        *LP(u32x2, l_scr) = u32x2{SENT, SENT};
      }
    }
  }
  {
    float t[4], r[2];
#pragma unroll
    for (int k = 0; k < 4; ++k) t[k] = sw16_add(o8[k], o8[k + 4]);
#pragma unroll
    for (int k = 0; k < 2; ++k) r[k] = sw32_add(t[k], t[k + 2]);
    if (!fin) { *LP(u32x2, l_scr) = u32x2{nosent(asu(r[0])), nosent(asu(r[1]))}; if (cw == 6) STAMP(33); }
    else {
      STAMP(13);
      u32x2 s[7]; int sp = 0;
      while (true) { FENCE(); unsigned worst = 0;
#pragma unroll
        for (int k = 0; k < 7; ++k) { s[k] = *(const LAS u32x2*)(L + L_SCR + k * 2048 + lane * 8); worst = umax(worst, umax(s[k][0], s[k][1])); }
        if (__all(worst != SENT) || dry || ++sp > SPIN_CAP) break; }
      r[0] += ((asf(s[0][0]) + asf(s[1][0])) + (asf(s[2][0]) + asf(s[3][0]))) + ((asf(s[4][0]) + asf(s[5][0])) + asf(s[6][0]));
      r[1] += ((asf(s[0][1]) + asf(s[1][1])) + (asf(s[2][1]) + asf(s[3][1]))) + ((asf(s[4][1]) + asf(s[5][1])) + asf(s[6][1]));
      const unsigned v = nosent(pk(r[0] * inv, r[1] * inv));
      auto y = __builtin_amdgcn_permlane32_swap(v, v, false, false);
      auto z0 = __builtin_amdgcn_permlane16_swap(y[0], y[0], false, false), z1 = __builtin_amdgcn_permlane16_swap(y[1], y[1], false, false);
      st16(u32x4{z0[0], z1[0], z0[1], z1[1]}, rt, a_rec);
      STAMP(7);
    }
  }
  WAIT_LANDED(1024, 1);
  if (r_wo || dry) {
    unsigned le; int sp = 0;
    while (true) { FENCE(); le = ld4(rt, a_w); if (__all(le != SENT) || ++sp > SPIN_CAP) break; __builtin_amdgcn_s_sleep(SLW1); }
    const float ls = asf(le);
    float M = fmaxf(ls, dpp<X1>(ls)); M = fmaxf(M, dpp<X2>(M));
    const float e = ex2(ls - M); float S = e + dpp<X1>(e); S += dpp<X2>(S);
    float wt = e * rcp(S); PIN(wt);
    if (cw == 0) STAMP(14);
    if (BEAC) { int t = 0; if (lane == 0 && !dry) t = __hip_atomic_fetch_add((LAS int*)(L + L_CTL + 12), 1, __ATOMIC_RELAXED, __HIP_MEMORY_SCOPE_WORKGROUP); if (__builtin_amdgcn_readfirstlane(t) == 7 || dry) st4(0u, rt, a_bc); }
    if (!dry) pause<DEL1>();
    u32x4 B[4]; sp = 0;
    while (true) {
      FENCE();
#pragma unroll
      for (int i = 0; i < 4; ++i) B[i] = ld16(rt, a_p + i * 64);
      if (__all(umax(umax(mx4(B[0]), mx4(B[1])), umax(mx4(B[2]), mx4(B[3]))) != SENT) || ++sp > SPIN_CAP) break;
      __builtin_amdgcn_s_sleep(SLP);
    }
    if (cw == 0) STAMP(15);
    f32x4 c0 = {0.f, 0.f, 0.f, 0.f}, c1 = {0.f, 0.f, 0.f, 0.f};
#pragma unroll
    for (int i = 0; i < 4; ++i) { c0 = mfma(FB[2 * i], B[i], c0); c1 = mfma(FB[2 * i + 1], B[i], c1); }
    f32x4 c;
#pragma unroll
    for (int r = 0; r < 4; ++r) { const float u0 = c0[r] * wt, u1 = c1[r] * wt; const float v0 = u0 + dpp<X1>(u0), v1 = u1 + dpp<X1>(u1); const float v = (col & 1) ? v1 : v0; c[r] = v + dpp<X2>(v); }
    if ((col & 10) == 0 && !dry) *LP(f32x4, l_w) = c;
    if (!dry) BAR();
    if (cw == 0) {
      const f32x4 s = *LP(f32x4, l_r) + *LP(f32x4, l_r + 256);
      const float t0 = sw16_add(s[0], s[1]), t1 = sw16_add(s[2], s[3]);
      auto y0 = __builtin_amdgcn_permlane32_swap(asu(t0), asu(t0), false, false), y1 = __builtin_amdgcn_permlane32_swap(asu(t1), asu(t1), false, false);
      const unsigned q0 = nosent(asu(asf(y0[0]) + asf(y0[1]))), q1 = nosent(asu(asf(y1[0]) + asf(y1[1])));
      auto z0 = __builtin_amdgcn_permlane16_swap(q0, q0, false, false), z1 = __builtin_amdgcn_permlane16_swap(q1, q1, false, false);
      st16(u32x4{z0[0], z0[1], z1[0], z1[1]}, rt, a_s);
      STAMP(16);
    }
  }
  if (r_gu || dry) {
    if (BEAC) {
      if (cw == 0) { int sp = 0;
        while (true) { FENCE(); const unsigned bc = ld4(rt, a_w); if (__all(bc != SENT) || ++sp > SPIN_CAP) break; __builtin_amdgcn_s_sleep(SLW2); }
        STAMP(25);
        if (!dry) pause<DEL2>(); }
      if (!dry) BAR();
    }
    if (cw < 5) {
      const u32x2 xv = *LP(u32x2, l_hw);
      int sp = 0;
      u32x4 P[4]; sp = 0;
      while (true) {
        FENCE();
#pragma unroll
        for (int i = 0; i < 4; ++i) P[i] = __builtin_amdgcn_raw_buffer_load_b128(rt, a_p, i * 2 * D * 4, AUX_SC1);
        if (__all(umax(umax(mx4(P[0]), mx4(P[1])), umax(mx4(P[2]), mx4(P[3]))) != SENT) || ++sp > SPIN_CAP) break;
        __builtin_amdgcn_s_sleep(SLP);
      }
      if (BEAC && cw == 0) st4(0u, rt, a_bc);
      if (cw == 0) STAMP(17);
      float hv[4];
#pragma unroll
      for (int k = 0; k < 4; ++k) { const float s = (asf(P[0][k]) + asf(P[1][k])) + (asf(P[2][k]) + asf(P[3][k]));
        hv[k] = ((k & 1) ? hi(xv[k >> 1]) : lo(xv[k >> 1])) + rb(s); }
      if (!dry) *LP(u32x2, l_hw) = u32x2{pk(hv[0], hv[1]), pk(hv[2], hv[3])};
    }
    if (!dry) BAR();
    f32x4 c = {0.f, 0.f, 0.f, 0.f}, st = {0.f, 0.f, 0.f, 0.f};
#pragma unroll
    for (int i = 0; i < 5; ++i) { const u32x4 hf = *LP(u32x4, l_h + i * 64); c = mfma(FB[i], hf, c); st = mfma(hf, hf, st); }
    if (col < 3 && !dry) *LP(f32x4, l_w) = c;
    if (lane < 2 && !dry) *LP(f32x4, l_w + 192 - lane * 48) = st;
    if (!dry) BAR();
    if (cw == 0) {
      const int bb = kq & 1;
      float s = 0.f, cs = 0.f, s1 = 0.f, s2 = 0.f;
      const unsigned ls_ = l_r - col * 4 + 192 + bb * 16;
#pragma unroll
      for (int k = 0; k < 4; ++k) { s += *LP(float, l_r + bb * 64 + k * 512); cs += *LP(float, l_r + 128 + k * 512); s1 += *LP(float, ls_ + 8 + k * 512); s2 += *LP(float, ls_ + bb * 4 + k * 512); }
      const float mean = s1 * (1.f / D), var = fmaxf(fmaf(-mean, mean, s2 * (1.f / D)), 0.f);
      const float den = rb(__builtin_amdgcn_sqrtf(rb(rb(var) + 1e-5f)));
      const float y = rb(fmaf(-rb(mean), cs, s) * rcp(den));
      auto r = __builtin_amdgcn_permlane32_swap(asu(y), asu(y), false, false);
      const float gt = asf(r[0]), up = asf(r[1]);
      const float u = 0.7978845608028654f * fmaf(0.044715f * gt * gt, gt, gt);
      const float ge = rb(gt * rcp(1.f + ex2(-2.f * L2E * u)));
      const float gv = ge * up;
      const unsigned v = nosent(pk(gv, dpp<X1>(gv)));
      st16(u32x4{v, dppu<0x102>(v), dppu<0x104>(v), dppu<0x106>(v)}, rt, a_s);
      STAMP(18);
    }
  }
  if (r_dn || dry) {
    if (BEAC) {
      if (cw == 0) { int sp = 0;
        while (true) { FENCE(); const unsigned bc = ld4(rt, a_w); if (__all(bc != SENT) || ++sp > SPIN_CAP) break; __builtin_amdgcn_s_sleep(SLW3); }
        STAMP(26);
        if (!dry) pause<DEL3>(); }
      if (!dry) BAR();
    }
    const unsigned xv = *(LAS u16*)(L + L_H + a_x * 2);
    const int a_r = cw == 0 ? WS_HP + a_x * 4 : OOB - 16384;
    u32x4 B[7]; unsigned P[4]; int sp = 0;
    while (true) {
      FENCE();
#pragma unroll
      for (int i = 0; i < 7; ++i) B[i] = ld16(rt, (i < 5 ? a_p : a_p2) + i * 256);
#pragma unroll
      for (int i = 0; i < 4; ++i) P[i] = __builtin_amdgcn_raw_buffer_load_b32(rt, a_r, i * 2 * D * 4, AUX_SC1);
      unsigned worst = umax(umax(P[0], P[1]), umax(P[2], P[3]));
#pragma unroll
      for (int i = 0; i < 7; ++i) worst = umax(worst, mx4(B[i]));
      if (__all(worst != SENT) || ++sp > SPIN_CAP) break;
      __builtin_amdgcn_s_sleep(SLP);
    }
    const float hres = rb(lo(xv) + rb((asf(P[0]) + asf(P[1])) + (asf(P[2]) + asf(P[3]))));
    if (cw == 0) STAMP(19);
    f32x4 c0 = {0.f, 0.f, 0.f, 0.f}, c1 = {0.f, 0.f, 0.f, 0.f};
#pragma unroll
    for (int i = 0; i < 7; ++i) { if (i & 1) c1 = mfma(FB[i], B[i], c1); else c0 = mfma(FB[i], B[i], c0); }
    if (col < 2 && !dry) *LP(f32x4, l_w) = c0 + c1;
    if (!dry) BAR();
    if (cw == 0) {
      float s = (*LP(float, l_r) + *LP(float, l_r + 256)) + (*LP(float, l_r + 512) + *LP(float, l_r + 768));
      s = sw32_add(s, s);
      const float y = hres + rb(s);
      __builtin_amdgcn_raw_buffer_store_b32(pk(y, dpp<X1>(y)), mk(out, dry ? 0 : 2 * D * 2), a_s, 0, 0);
      if (dn_cg == 0) __builtin_amdgcn_raw_buffer_store_b64(u32x2{tagA, tagB}, rt, lane == 0 ? WS_EPOCH : OOB, 0, AUX_SC1);
      STAMP(20);
    }
  }
  if (dry) {
    STAMP(21);
    int sp = 0;
    while (true) {
      FENCE();
      const u32x4 f0 = ld16(rws, WS_FLAG + lane * 16), f1 = ld16(rws, WS_FLAG + 1024 + lane * 16);
      if (__all(f0[0] == tagA && f0[1] == tagB && f0[2] == tagA && f0[3] == tagB && f1[0] == tagA && f1[1] == tagB && f1[2] == tagA && f1[3] == tagB) || ++sp > SPIN_CAP) break;
      __builtin_amdgcn_s_sleep(8);
    }
    ctl[2] = 1;
  }
  STAMP_OUT();
}

extern "C" void kernel_launch(void* const* d_in, const int* in_sizes, int n_in, void* d_out, int out_size, void* d_ws, size_t ws_size, hipStream_t stream) {
  (void)hipFuncSetAttribute((const void*)k_block, hipFuncAttributeMaxDynamicSharedMemorySize, L_END);
  hipLaunchKernelGGL(k_block, dim3(NWG), dim3(NTHR), L_END, stream, (const u16*)d_in[0], (const char*)d_in[1], (const char*)d_in[2], (const u16*)d_in[3], (const u16*)d_in[4],
                     (const u16*)d_in[6], (const u16*)d_in[7], (const u16*)d_in[8], (u16*)d_out, (char*)d_ws);
}
```

```cpp
#include <hip/hip_runtime.h>
#include <stdint.h>

constexpr int WB = 21, WLO = 16, WHI = 30, WEND = 28, GAIN = 14, WN0 = 4, SLP = 1, CODEKB = 20;

#define LAS __attribute__((address_space(3)))
typedef unsigned u32x4 __attribute__((ext_vector_type(4)));
typedef unsigned u32x2 __attribute__((ext_vector_type(2)));
typedef float f32x4 __attribute__((ext_vector_type(4)));
typedef __bf16 bf16x2 __attribute__((ext_vector_type(2)));
typedef __bf16 bf16x8 __attribute__((ext_vector_type(8)));
typedef short s16x4 __attribute__((ext_vector_type(4)));
typedef unsigned short u16;
typedef unsigned long long u64;
typedef __amdgpu_buffer_rsrc_t rsrc_t;

constexpr int D = 640, NH = 32, DH = 128, F = 1728, HD = NH * DH;
constexpr int NWG = 256, NTHR = 640;
constexpr int WS_EPOCH = 0, WS_HP = 8192, WS_G = 32768, WS_REC = 49152, WS_LSE = 114688, WS_TS = 147456, WS_QP = 212992, WS_END = WS_QP + 524288;
constexpr int NSLOT = 17, RING = NSLOT * 8192;
constexpr int L_SCR = RING;
constexpr int L_X = L_SCR + 16384;
constexpr int L_RED = 0;
constexpr int L_Q = L_X + 2560;
constexpr int L_CTL = L_Q + 336;
constexpr int L_CNT = L_CTL + 128;
constexpr int L_TCK = L_CNT + 256;
constexpr int L_END = L_TCK + 64;
static_assert(L_END <= 163840, "LDS");
constexpr unsigned SENT = 0xffffffffu;
constexpr int OOB = 0x7fffffff;
constexpr float L2E = 1.4426950408889634f;
constexpr int SPIN_CAP = 4000000;
constexpr int AUX_SC1 = 16;

__device__ __forceinline__ float rb(float f) { return (float)(__bf16)f; }
__device__ __forceinline__ float lo(unsigned v) { return __uint_as_float(v << 16); }
__device__ __forceinline__ float hi(unsigned v) { return __uint_as_float(v & 0xffff0000u); }
__device__ __forceinline__ float asf(unsigned v) { return __uint_as_float(v); }
__device__ __forceinline__ unsigned asu(float v) { return __float_as_uint(v); }
__device__ __forceinline__ unsigned pk(float a, float b) { bf16x2 r = {(__bf16)a, (__bf16)b}; return __builtin_bit_cast(unsigned, r); }
__device__ __forceinline__ unsigned umin(unsigned a, unsigned b) { return a < b ? a : b; }
__device__ __forceinline__ unsigned umax(unsigned a, unsigned b) { return a > b ? a : b; }
__device__ __forceinline__ unsigned nosent(unsigned v) { return umin(v, SENT - 1); }
__device__ __forceinline__ unsigned mx4(u32x4 v) { return umax(umax(v[0], v[1]), umax(v[2], v[3])); }
__device__ __forceinline__ float dot2(unsigned a, unsigned b, float c) { return __builtin_amdgcn_fdot2_f32_bf16(__builtin_bit_cast(bf16x2, a), __builtin_bit_cast(bf16x2, b), c, false); }
template <int CTRL> __device__ __forceinline__ float dpp(float x) { return __builtin_bit_cast(float, __builtin_amdgcn_mov_dpp(__builtin_bit_cast(int, x), CTRL, 0xf, 0xf, true)); }
template <int CTRL> __device__ __forceinline__ unsigned dppu(unsigned x) { return (unsigned)__builtin_amdgcn_mov_dpp((int)x, CTRL, 0xf, 0xf, true); }
constexpr int X1 = 0xB1, X2 = 0x4E, X7 = 0x141, R8 = 0x128, R4 = 0x124, R2 = 0x122, R1 = 0x121;
__device__ __forceinline__ float row_sum16(float x) { x += dpp<R8>(x); x += dpp<R4>(x); x += dpp<R2>(x); x += dpp<R1>(x); return x; }
__device__ __forceinline__ float sw16_add(float a, float b) { auto r = __builtin_amdgcn_permlane16_swap(asu(a), asu(b), false, false); return asf(r[0]) + asf(r[1]); }
__device__ __forceinline__ float sw32_add(float a, float b) { auto r = __builtin_amdgcn_permlane32_swap(asu(a), asu(b), false, false); return asf(r[0]) + asf(r[1]); }
__device__ __forceinline__ float sw16_max(float a) { auto r = __builtin_amdgcn_permlane16_swap(asu(a), asu(a), false, false); return fmaxf(asf(r[0]), asf(r[1])); }
__device__ __forceinline__ float sw32_max(float a) { auto r = __builtin_amdgcn_permlane32_swap(asu(a), asu(a), false, false); return fmaxf(asf(r[0]), asf(r[1])); }
__device__ __forceinline__ float wave_sum(float x) { x = row_sum16(x); x = sw16_add(x, x); return sw32_add(x, x); }
__device__ __forceinline__ float ex2(float x) { return __builtin_amdgcn_exp2f(x); }
__device__ __forceinline__ float rcp(float x) { return __builtin_amdgcn_rcpf(x); }
__device__ __forceinline__ rsrc_t mk(const void* p, int n) { return __builtin_amdgcn_make_buffer_rsrc((void*)p, 0, n, 0x00020000); }
__device__ __forceinline__ f32x4 mfma(u32x4 a, u32x4 b, f32x4 c) { return __builtin_amdgcn_mfma_f32_16x16x32_bf16(__builtin_bit_cast(bf16x8, a), __builtin_bit_cast(bf16x8, b), c, 0, 0, 0); }
__device__ __forceinline__ u32x4 ld16(rsrc_t r, int o) { return __builtin_amdgcn_raw_buffer_load_b128(r, o, 0, AUX_SC1); }
__device__ __forceinline__ unsigned ld4(rsrc_t r, int o) { return __builtin_amdgcn_raw_buffer_load_b32(r, o, 0, AUX_SC1); }
__device__ __forceinline__ void st16(u32x4 v, rsrc_t r, int o) { __builtin_amdgcn_raw_buffer_store_b128(v, r, o, 0, AUX_SC1); }
__device__ __forceinline__ void st4(unsigned v, rsrc_t r, int o) { __builtin_amdgcn_raw_buffer_store_b32(v, r, o, 0, AUX_SC1); }
#define LGKM0() asm volatile("s_waitcnt lgkmcnt(0)" ::: "memory")
#define FENCE() asm volatile("" ::: "memory")
#define VM0() asm volatile("s_waitcnt vmcnt(0)" ::: "memory")
#define BAR() do { LGKM0(); __builtin_amdgcn_s_barrier(); FENCE(); } while (0)
#define PIN(x) asm volatile("" : "+v"(x))

#define LD1(j, base, off) "s_waitcnt vmcnt(%c[W])\n\t.if %c[P] == " #j "\n\tds_write_b32 %[ca], %[tmp]\n\t.endif\n\tglobal_load_lds_dwordx4 %[vo], " base " offset:" #off " nt\n\t"
#define LD4(base) LD1(0, base, 0) LD1(1, base, 1024) LD1(2, base, 2048) LD1(3, base, 3072)
#define LHEAD "s_mov_b32 %[keep], m0\n1:\n\ts_mov_b32 m0, %[lds]\n\ts_sub_u32 %[n], %[n], 1\n\t"
#define LTAIL "v_add_u32 %[vo], 0x2000, %[vo]\n\ts_add_u32 %[lds], %[lds], 0x2000\n\tv_and_b32 %[vo], 0x7ffff, %[vo]\n\ts_cmp_ge_u32 %[lds], %[ring]\n\ts_cselect_b32 %[ts], %[rsz], 0\n\t" \
              "s_sub_u32 %[lds], %[lds], %[ts]\n\ts_cmp_lg_u32 %[n], 0\n\ts_cbranch_scc1 1b\n\ts_mov_b32 m0, %[keep]"
#define LOUT [keep] "=&s"(keep), [ts] "=&s"(ts), [tmp] "=&v"(tmp), [n] "+s"(n), [vo] "+v"(voff), [lds] "+s"(lds), [cnt] "+v"(cnt)
#define LIN [pa] "s"(pa), [ca] "v"(ca), [ring] "s"(ring), [rsz] "s"(RING), [W] "i"(W), [P] "i"(W & 3), [D] "i"(W & ~3)
template <int W> __device__ __forceinline__ void ld_slots(int n, const char* pa, unsigned& voff, unsigned& lds, unsigned ring, unsigned ca, int& cnt) {
  unsigned keep, ts; int tmp;
  asm volatile(LHEAD "v_subrev_u32 %[tmp], %c[D], %[cnt]\n\tv_add_u32 %[cnt], 8, %[cnt]\n\t" LD4("%[pa]") "s_add_u32 m0, m0, 0x1000\n\tv_add_u32 %[tmp], 4, %[tmp]\n\t" LD4("%[pb]") LTAIL
     : LOUT : [pb] "s"(pa + 4096), LIN : "memory", "scc");
}
template <int W> __device__ __forceinline__ void ld_halves(const char* pa, unsigned voff, unsigned lds, unsigned ring, unsigned ca, int& cnt) {
  int n = 8; unsigned keep, ts; int tmp;
  asm volatile(LHEAD "v_subrev_u32 %[tmp], %c[D], %[cnt]\n\tv_add_u32 %[cnt], 4, %[cnt]\n\t" LD4("%[pa]") LTAIL : LOUT : LIN : "memory", "scc");
}
#define LS1(i, off) "s_mov_b32 m0, %[l" #i "]\n\ts_waitcnt vmcnt(%c[W])\n\tglobal_load_lds_dwordx4 %[v" #i "], %[pa] offset:" #off " nt\n\t"
#define LS8(off) "v_add_u32 %[cnt], 8, %[cnt]\n\t" LS1(0, off) "ds_write_b32 %[ca], %[cnt]\n\t" LS1(1, off) LS1(2, off) LS1(3, off) LS1(4, off) LS1(5, off) LS1(6, off) LS1(7, off)
template <int W> __device__ __forceinline__ void ld_singles(const char* pa, const unsigned (&vo)[8], const unsigned (&ld)[8], unsigned ca, int cnt) {
  unsigned keep; cnt -= W + 8;
  asm volatile("s_mov_b32 %[keep], m0\n\ts_nop 1\n\t" LS8(0) LS8(1024) LS8(2048) LS8(3072) "s_mov_b32 m0, %[keep]"
     : [keep] "=&s"(keep), [cnt] "+v"(cnt)
     : [pa] "s"(pa), [ca] "v"(ca), [W] "i"(W), [v0] "v"(vo[0]), [v1] "v"(vo[1]), [v2] "v"(vo[2]), [v3] "v"(vo[3]), [v4] "v"(vo[4]), [v5] "v"(vo[5]), [v6] "v"(vo[6]), [v7] "v"(vo[7]),
       [l0] "s"(ld[0]), [l1] "s"(ld[1]), [l2] "s"(ld[2]), [l3] "s"(ld[3]), [l4] "s"(ld[4]), [l5] "s"(ld[5]), [l6] "s"(ld[6]), [l7] "s"(ld[7]) : "memory");
}
__device__ __forceinline__ unsigned uni(unsigned v) { return __builtin_amdgcn_readfirstlane(v); }
__device__ __forceinline__ const char* unip(const char* p) { const u64 v = (u64)p; return (const char*)(((u64)uni((unsigned)(v >> 32)) << 32) | uni((unsigned)v)); }

__global__ void __launch_bounds__(NTHR, 3) k_block(const u16* __restrict__ x, const char* __restrict__ K, const char* __restrict__ V, const u16* __restrict__ wq,
                                                const u16* __restrict__ wo, const u16* __restrict__ wg, const u16* __restrict__ wu, const u16* __restrict__ wd,
                                                u16* __restrict__ out, char* __restrict__ ws) {
  extern __shared__ __attribute__((aligned(16))) char lds_raw[];
  LAS char* L = (LAS char*)lds_raw;
  volatile LAS int* ctl = (volatile LAS int*)(L + L_CTL);
  const int lane = threadIdx.x & 63, w = __builtin_amdgcn_readfirstlane(threadIdx.x >> 6);
  const int g = blockIdx.x;
  asm volatile("" :: "s"(x), "s"(K), "s"(V), "s"(wq), "s"(wo), "s"(wg), "s"(wu), "s"(wd), "s"(out), "s"(ws));
  const unsigned rot = (g * 2654435761u) >> 29;
  unsigned voff = lane * 16 + rot * 8192;
  const char* pa = K + (size_t)g * 524288;
  if (w == 0) {
    unsigned keep;
#define F4(p) "global_load_lds_dwordx4 %1, " p " nt\n\tglobal_load_lds_dwordx4 %1, " p " offset:1024 nt\n\tglobal_load_lds_dwordx4 %1, " p " offset:2048 nt\n\tglobal_load_lds_dwordx4 %1, " p " offset:3072 nt\n\ts_add_u32 m0, m0, 0x1000\n\t"
#define F8 "s_nop 0\n\t" F4("%2") "s_nop 0\n\t" F4("%3") "v_add_u32 %1, 0x2000, %1\n\tv_and_b32 %1, 0x7ffff, %1\n\t"
    asm volatile("s_mov_b32 %0, m0\n\ts_mov_b32 m0, %4\n\t" F8 F8 F8 "s_mov_b32 m0, %0" : "=&s"(keep), "+v"(voff) : "s"(pa), "s"(pa + 4096), "s"((unsigned)(size_t)L) : "memory", "scc");
  }
  int w2 = w; asm volatile("" : "+s"(w2));
  if (w2 == 0) {
    __builtin_amdgcn_s_barrier(); FENCE();
    __builtin_amdgcn_s_setprio(3);
    const unsigned lb = (unsigned)(size_t)L, ca = lb + L_CNT + lane * 4;
    unsigned lo_ = lb + 3 * 8192;
    int cnt = 24, wq_ = WB;
#define WSEL(X) switch (wq_) { case 16: X(16); break; case 17: X(17); break; case 18: X(18); break; case 19: X(19); break; case 20: X(20); break; \
      case 21: X(21); break; case 22: X(22); break; case 23: X(23); break; case 24: X(24); break; case 25: case 26: X(25); break; case 27: case 28: X(27); break; default: X(29); break; }
    const unsigned chk = lb + L_CTL + (lane < 8 ? 32 + ((lane - 1) & 7) * 4 : 24);
#pragma unroll 1
    for (int s = 3; s <= 120;) {
      const int r = s >> 3;
      int n = 8 - (s & 7);
      if (s > 3) {
        int sp = 0;
        while (true) {
          u64 now; int v;
          asm volatile("s_memrealtime %0\n\tds_read_b32 %1, %2\n\ts_waitcnt lgkmcnt(0)" : "=&s"(now), "=&v"(v) : "v"(chk) : "memory");
          if ((s & 7) == 0 && sp == 0) *(volatile LAS unsigned*)(L + L_TCK + r * 4) = (unsigned)now | 1u;
          wq_ = WB + __builtin_amdgcn_readlane(v, 8);
          const unsigned ok = (unsigned)__builtin_amdgcn_ballot_w64(v >= r - (lane == 0 ? 2 : 1)) | 0xffffff00u;
          const int run = __builtin_ctz(~(ok >> (s & 7)) | 0x100u);
          if (run >= (s == 120 ? 8 : 1) || ++sp > SPIN_CAP) { n = run > 0 && run < n ? run : n; break; }
          __builtin_amdgcn_s_sleep(1);
        }
      }
      if (s == 120) break;
      if (s == 64) pa = V + (size_t)g * 524288;
      pa = unip(pa); lo_ = uni(lo_);
#define XS(W) ld_slots<W>(uni(n), pa, voff, lo_, lb + RING, ca, cnt)
      WSEL(XS)
      s += n;
    }
    unsigned vo8[8], ld8[8];
#pragma unroll
    for (int i = 0; i < 8; ++i) { vo8[i] = (voff + i * 8192) & 0x7ffff; ld8[i] = lb + ((120 + i) % NSLOT) * 8192 + 4096; PIN(vo8[i]); }
    ld_halves<WEND>(unip(pa), voff, uni(lo_), lb + RING, ca, cnt);
    ld_singles<WEND>(unip(pa + 4096), vo8, ld8, ca, cnt);
#define DR(n) if (n <= WEND) { asm volatile("s_waitcnt vmcnt(" #n ")" ::: "memory"); *(volatile LAS int*)(L + L_CNT) = 1024 - n; }
    DR(24) DR(22) DR(20) DR(18) DR(16) DR(14) DR(12) DR(10) DR(8) DR(7) DR(6) DR(5) DR(4) DR(3) DR(2) DR(1) DR(0)
#undef DR
    return;
  }
  const int bh = g >> 2, sp4 = g & 3, b = bh >> 5, h = bh & 31;
  const int cw = w - 2, col = lane & 15, kq = lane >> 4;
  const int j = b * 4 + sp4, row0 = 80 * j + 10 * cw;
  const bool hlp = w == 1;
  u32x4 wv[3]; unsigned xb[2] = {0, 0}, pft = 0; u32x2 ep = {0, 0};
  {
    const rsrc_t r0 = mk(hlp ? x : wq + (size_t)row0 * HD + h * DH, hlp ? 2 * D * 2 : 9 * HD * 2 + 256);
    const int o0 = hlp ? lane * 16 : kq * (HD * 2) + col * 16, o1 = o0 + (hlp ? 1280 : 4 * HD * 2), o2 = hlp ? OOB : o0 + 8 * HD * 2;
    asm volatile("s_nop 4\n\tbuffer_load_dwordx4 %0, %3, %6, 0 offen\n\tbuffer_load_dwordx4 %1, %4, %6, 0 offen\n\tbuffer_load_dwordx4 %2, %5, %6, 0 offen"
                 : "=&v"(wv[0]), "=&v"(wv[1]), "=&v"(wv[2]) : "v"(o0), "v"(o1), "v"(o2), "s"(r0) : "memory");
    if (hlp) {
      u64 pc; asm volatile("s_getpc_b64 %0" : "=s"(pc));
      asm volatile("s_nop 4\n\tbuffer_load_dword %0, %4, %5, 0 offen offset:1024\n\tbuffer_load_dword %1, %4, %5, 0 offen offset:2304\n\tbuffer_load_dwordx2 %2, off, %6, 0 sc1\n\t"
                   "buffer_load_dword %3, %7, %8, 0 offen"
                   : "+v"(xb[0]), "+v"(xb[1]), "+v"(ep), "+v"(pft)
                   : "v"(lane * 4), "s"(r0), "s"(mk(ws, 8)), "v"((g >> 3) * 4096 + lane * 64), "s"(mk((const void*)(pc & ~255ull), CODEKB * 1024)) : "memory");
    }
  }
  {
    const int t = threadIdx.x - 64;
    *(LAS u32x4*)(L + L_SCR + (t >> 6) * 2048 + (t & 63) * 16) = u32x4{SENT, SENT, SENT, SENT};
    const unsigned v = t < 21 ? SENT : 0;
    if (t < 49) *(LAS u32x4*)(L + L_Q + t * 16) = u32x4{v, v, v, v};
  }
  BAR();
  const int xs = g & 7, a = g >> 3;
  const bool r_wo = a < 10;
  const int wo_t = xs * 10 + a, wo_kg = wo_t / 20, wo_cg = wo_t % 20;
  int gu_quad = -1; if (a >= 10 && a < 22) gu_quad = xs + 8 * ((a - 10) >> 2); else if (a >= 22 && a < 26 && xs < 3) gu_quad = xs + 24;
  const bool r_gu = gu_quad >= 0; const int gu_cg = gu_quad * 4 + ((a - 10) & 3);
  int dn_quad = -1; if (a >= 26 && a < 30) dn_quad = xs; else if (a >= 22 && a < 26 && xs >= 6) dn_quad = xs + 2;
  const bool r_dn = dn_quad >= 0; const int dn_cg = dn_quad * 4 + ((a - 10) & 3);

  const rsrc_t rt = mk(ws, WS_END);
  if (w == 1) {
    asm volatile("s_waitcnt vmcnt(1)" : "+v"(wv[0]), "+v"(wv[1]), "+v"(wv[2]), "+v"(xb[0]), "+v"(xb[1]), "+v"(ep) :: "memory");
    const u32x4 xa[2] = {wv[0], wv[1]}; const u32x2 e = ep;
    float mb[2], den[2];
#pragma unroll
    for (int bb = 0; bb < 2; ++bb) {
      *(LAS u32x4*)(L + L_X + bb * 1280 + lane * 16) = xa[bb]; *(LAS unsigned*)(L + L_X + bb * 1280 + 1024 + lane * 4) = xb[bb];
      float v[10];
#pragma unroll
      for (int k = 0; k < 4; ++k) { v[2 * k] = lo(xa[bb][k]); v[2 * k + 1] = hi(xa[bb][k]); }
      v[8] = lo(xb[bb]); v[9] = hi(xb[bb]);
      float s = ((v[0] + v[1]) + (v[2] + v[3])) + ((v[4] + v[5]) + (v[6] + v[7])) + (v[8] + v[9]);
      const float mean = wave_sum(s) * (1.f / D);
      float q0 = 0.f, q1 = 0.f;
#pragma unroll
      for (int k = 0; k < 5; ++k) { const float d0 = v[2 * k] - mean, d1 = v[2 * k + 1] - mean; q0 = fmaf(d0, d0, q0); q1 = fmaf(d1, d1, q1); }
      const float var = wave_sum(q0 + q1) * (1.f / D);
      mb[bb] = rb(mean); den[bb] = rb(sqrtf(rb(rb(var) + 1e-5f)));
    }
    const unsigned tagA = e[0] + 1, tagB = e[1] + 1;
    if (lane == 0) { *(LAS f32x4*)(L + L_CTL + 64) = f32x4{mb[0], den[0], mb[1], den[1]}; *(LAS u32x2*)(L + L_CTL + 80) = u32x2{tagA, tagB}; FENCE(); ctl[1] = 1; }
    int o = OOB;
    if (lane < 16) o = WS_REC + g * 256 + lane * 16;
    else if (lane < 32) { if (r_wo) o = WS_HP + ((wo_kg * 2 + ((lane >> 3) & 1)) * D + wo_cg * 32 + (lane & 7) * 4) * 4; }
    else if (lane < 36) { if (r_gu) o = WS_G + gu_cg * 128 + (lane & 3) * 16; }
    st16(u32x4{SENT, SENT, SENT, SENT}, rt, o);
    st4(SENT, rt, lane == 0 ? WS_LSE + g * 128 : OOB);
    VM0(); PIN(pft);
    __builtin_amdgcn_raw_buffer_store_b64(u32x2{tagA, tagB}, rt, lane == 0 ? WS_LSE + g * 128 + 8 : OOB, 0, AUX_SC1);
    unsigned t1 = 0, tp = 0; float Ls = 1.f;
#pragma unroll 1
    for (int c = 1; c < 16; ++c) {
      unsigned tc; int sp = 0;
      while ((tc = uni(*(volatile LAS unsigned*)(L + L_TCK + c * 4))) == 0 && ++sp < SPIN_CAP) __builtin_amdgcn_s_sleep(4);
      st4(tc, rt, (lane == 0 && (a & 7) == xs) ? WS_TS + (c * 32 + (a >> 3) * 8 + xs) * 128 : OOB);
      if (c == 1) t1 = tc - 1000;
      else {
        const unsigned v = ld4(rt, lane < 32 ? WS_TS + ((c - 1) * 32 + lane) * 128 : OOB);
        const float d = lane < 32 ? (float)(int)((v - t1 < tc - t1 ? v : tc) - tp) : 0.f;
        const float lag = wave_sum(d) * (-1.f / 8192.f);
        int adj = (int)__builtin_rintf(lag * GAIN);
        adj = adj < WLO - WB ? WLO - WB : adj > WHI - WB ? WHI - WB : adj;
        ctl[6] = adj;
      }
      tp = tc;
      if (c == 9) {
        u32x4 t0, t1_, t2, t3; sp = 0;
        while (true) { FENCE(); t0 = *(const LAS u32x4*)(L + L_Q + 256); t1_ = *(const LAS u32x4*)(L + L_Q + 272); t2 = *(const LAS u32x4*)(L + L_Q + 288); t3 = *(const LAS u32x4*)(L + L_Q + 304);
          if (__all(umax(mx4(t2), mx4(t3)) != SENT) || ++sp > SPIN_CAP) break; __builtin_amdgcn_s_sleep(2); }
        const float M = fmaxf(fmaxf(fmaxf(asf(t0[0]), asf(t0[1])), fmaxf(asf(t0[2]), asf(t0[3]))), fmaxf(fmaxf(asf(t1_[0]), asf(t1_[1])), fmaxf(asf(t1_[2]), asf(t1_[3]))));
        Ls = ((asf(t2[0]) + asf(t2[1])) + (asf(t2[2]) + asf(t2[3]))) + ((asf(t3[0]) + asf(t3[1])) + (asf(t3[2]) + asf(t3[3])));
        st4(nosent(asu(fmaf(M, L2E, __builtin_amdgcn_logf(Ls)))), rt, lane == 0 ? WS_LSE + g * 128 : OOB);
      }
      if (c == 12) {
        unsigned le; sp = 0;
        while (true) { FENCE(); le = ld4(rt, WS_LSE + ((g & ~3) + ((lane + sp4) & 3)) * 128); if (__all(le != SENT) || ++sp > SPIN_CAP) break; __builtin_amdgcn_s_sleep(8); }
        const float ls = asf(le);
        float M = fmaxf(ls, dpp<X1>(ls)); M = fmaxf(M, dpp<X2>(M));
        const float e_ = ex2(ls - M); const float e0 = dpp<0x00>(e_), e1 = dpp<0x55>(e_), e2 = dpp<0xAA>(e_), e3 = dpp<0xFF>(e_);
        if (lane == 0) *(volatile LAS unsigned*)(L + L_Q + 320) = nosent(asu(e_ / (((e0 + e1) + (e2 + e3)) * Ls)));
      }
      if (c == 3 + (a & 3)) {
        sp = 0;
        while (true) {
          FENCE();
          bool ok = true;
#pragma unroll
          for (int k = 0; k < 4; ++k) { const u32x2 f = __builtin_amdgcn_raw_buffer_load_b64(rt, WS_LSE + 8 + lane * 128, k * 8192, AUX_SC1); ok = ok && f[0] == tagA && f[1] == tagB; }
          if (__all(ok) || ++sp > SPIN_CAP) break;
          __builtin_amdgcn_s_sleep(8);
        }
        ctl[2] = 1;
      }
    }
    __builtin_amdgcn_s_waitcnt(0);
    return;
  }
  LAS char* scr = L + L_SCR + cw * 2048;
  const unsigned lb = (unsigned)(size_t)L;
  int a_rec = kq == 0 ? WS_REC + g * 256 + col * 16 : OOB;
  const int gu_k0 = cw < 4 ? 96 * cw : 128 + 64 * cw, gu_nk = cw < 4 ? 96 : 64;
  int a_p, a_w, a_s;
  unsigned l_x, l_w, l_r, l_s;
  unsigned l_scr = (unsigned)(size_t)scr + lane * 8, l_h = (unsigned)(size_t)scr + 1024 + (r_gu ? (col < 2 ? col : 2) * 192 + kq * 16 : (col & 1) * 16 + kq * 256);
  if (r_wo) {
    a_p = WS_REC + ((((col >> 2) & 1) * 32 + wo_kg * 8 + cw) * 4 + (col & 3)) * 256 + (col >> 3) * 64 + kq * 16; a_w = 0;
    a_s = lane < 16 ? WS_HP + ((wo_kg * 2 + (lane >> 3)) * D + wo_cg * 32 + (lane & 7) * 4) * 4 : OOB;
    l_x = 0; l_w = lb + L_RED + 4 * (cw * 64 + (col >> 2) * 32 + (col & 1) * 16 + kq * 4); l_r = lb + L_RED + 4 * (kq * 128 + col * 4); l_s = 0;
  } else if (r_gu) {
    const int e = (lane >> 5) * D + gu_k0 + (lane & 31) * 4; const bool ok = (lane & 31) * 4 < gu_nk;
    a_p = ok ? WS_HP + e * 4 : OOB - 16384; a_w = (unsigned)(size_t)scr + (ok ? 1024 + (lane >> 5) * 192 + (lane & 31) * 8 : 1600);
    a_s = (lane < 32 && (lane & 7) == 0) ? WS_G + gu_cg * 128 + lane * 2 : OOB;
    l_x = lb + L_X + (ok ? e * 2 : 0); l_w = lb + L_RED + 4 * (col * 200 + kq * 48 + cw); l_r = lb + L_RED + 4 * (((lane >> 5) * 3 + (kq & 1)) * 200 + col * 12);
    l_s = lb + L_RED + 4 * (lane >> 5) * 600 + col * 48 + 1600;
  } else {
    const int e = (kq & 1) * D + dn_cg * 16 + col;
    a_p = (col >> 1) < (cw == 7 ? 5 : 7) ? WS_G + (cw * 14 + (col & 14) + (kq >> 1)) * 128 + (col & 1) * 32 + (kq & 1) * 16 : OOB;
    a_w = cw == 0 ? WS_HP + e * 4 : OOB - 16384;
    a_s = (lane < 32 && (lane & 1) == 0) ? e * 2 : OOB;
    l_x = lb + L_X + (r_dn ? e * 2 : 0); l_w = lb + L_RED + 4 * (cw * 64 + col * 16 + kq * 4); l_r = lb + L_RED + 4 * ((lane >> 5) * 256 + (lane & 31)); l_s = 0;
  }
  PIN(a_rec); PIN(a_p); PIN(a_w); PIN(a_s); PIN(l_x); PIN(l_w); PIN(l_r); PIN(l_s); PIN(l_scr); PIN(l_h);
#define LP(T, a) ((LAS T*)(size_t)(a))
  const int wstr = r_gu ? F * 2 : D * 2, wsz = r_wo ? 127 * D * 2 + 64 : r_gu ? (gu_nk - 1) * F * 2 + 32 : r_dn ? (cw == 7 ? 159 : 223) * D * 2 + 32 : 0;
  const size_t wbase = r_wo ? (size_t)(wo_kg * 1024 + cw * 128) * D + wo_cg * 32 : r_gu ? (size_t)gu_k0 * F + gu_cg * 16 : (size_t)(cw * 224) * D + dn_cg * 16;
  const u16* wpa = (r_wo ? wo : r_gu ? wg : wd) + wbase; const u16* wpb = (r_wo ? wo : r_gu ? wu : wd) + wbase;
  int woff = r_wo ? (lane >> 2) * wstr + (lane & 3) * 16 : (lane >> 1) * wstr + (lane & 1) * 16;
  const int wstepb = (r_wo ? 16 : 32) * wstr, wstepa = r_gu ? 0 : wstepb;
  const int wsteps = r_wo ? 8 : 2;
  const int trow = r_wo ? 64 : 32;
  int ta0 = (8 * kq + ((lane >> 2) & 3)) * trow + (lane & 3) * 8, ta1 = ta0 + (r_wo ? 32 : 1024); PIN(woff); PIN(ta0); PIN(ta1);
  u32x4 FB[8];
#pragma unroll
  for (int i = 0; i < 8; ++i) FB[i] = u32x4{0, 0, 0, 0};
  unsigned tagA, tagB;
  u32x4 qv, kk[8];
  int npre = 1;
  {
    { int sp = 0; while (ctl[1] == 0 && ++sp < SPIN_CAP) __builtin_amdgcn_s_sleep(0); }
    unsigned wr[3][4];
    asm volatile("s_waitcnt vmcnt(0)\n\tv_mov_b32 %0, %12\n\tv_mov_b32 %1, %13\n\tv_mov_b32 %2, %14\n\tv_mov_b32 %3, %15\n\tv_mov_b32 %4, %16\n\tv_mov_b32 %5, %17\n\tv_mov_b32 %6, %18\n\tv_mov_b32 %7, %19\n\tv_mov_b32 %8, %20\n\tv_mov_b32 %9, %21\n\tv_mov_b32 %10, %22\n\tv_mov_b32 %11, %23"
                 : "=&v"(wr[0][0]), "=&v"(wr[0][1]), "=&v"(wr[0][2]), "=&v"(wr[0][3]), "=&v"(wr[1][0]), "=&v"(wr[1][1]), "=&v"(wr[1][2]), "=&v"(wr[1][3]), "=&v"(wr[2][0]), "=&v"(wr[2][1]), "=&v"(wr[2][2]), "=&v"(wr[2][3])
                 : "v"(wv[0][0]), "v"(wv[0][1]), "v"(wv[0][2]), "v"(wv[0][3]), "v"(wv[1][0]), "v"(wv[1][1]), "v"(wv[1][2]), "v"(wv[1][3]), "v"(wv[2][0]), "v"(wv[2][1]), "v"(wv[2][2]), "v"(wv[2][3]) : "memory");
    {
      const f32x4 st = *(LAS f32x4*)(L + L_CTL + 64); const u32x2 tg = *(LAS u32x2*)(L + L_CTL + 80);
      tagA = tg[0]; tagB = tg[1];
      float acc[2][8];
#pragma unroll
      for (int bb = 0; bb < 2; ++bb) {
#pragma unroll
        for (int k = 0; k < 8; ++k) acc[bb][k] = 0.f;
#pragma unroll
        for (int i = 0; i < 3; ++i) { const float xn = rb(rb(lo(*(LAS u16*)(L + L_X + bb * 1280 + (row0 + (i == 2 ? 8 + (kq & 1) : 4 * i + kq)) * 2)) - st[2 * bb]) / st[2 * bb + 1]);
#pragma unroll
          for (int k = 0; k < 4; ++k) { acc[bb][2 * k] = fmaf(xn, lo(wr[i][k]), acc[bb][2 * k]); acc[bb][2 * k + 1] = fmaf(xn, hi(wr[i][k]), acc[bb][2 * k + 1]); } }
      }
      float u[8]; u32x4 t;
#pragma unroll
      for (int k = 0; k < 8; ++k) u[k] = sw32_add(acc[0][k], acc[1][k]);
#pragma unroll
      for (int k = 0; k < 4; ++k) t[k] = nosent(asu(sw16_add(u[k], u[k + 4])));
      *(LAS u32x4*)(scr + ((lane >> 5) * 128 + col * 8 + (kq & 1) * 4) * 4) = t;
    }
    {
      const LAS char* p = L + L_SCR + (kq * 2) * 2048 + (((lane >> 3) & 1) * 128 + 16 * cw + (lane & 7) * 2) * 4;
      u32x2 t0, t1; int sp = 0;
      while (true) {
        FENCE();
        t0 = *(const LAS u32x2*)p; t1 = *(const LAS u32x2*)(p + 2048);
        if (__all(umax(umax(t0[0], t0[1]), umax(t1[0], t1[1])) != SENT) || ++sp > SPIN_CAP) break;
      }
      float r0 = asf(t0[0]) + asf(t1[0]), r1 = asf(t0[1]) + asf(t1[1]);
      r0 = sw16_add(r0, r0); r1 = sw16_add(r1, r1); r0 = sw32_add(r0, r0); r1 = sw32_add(r1, r1);
      st16(u32x4{asu(r0), tagA, asu(r1), tagB}, rt, lane < 16 ? WS_QP + (((h * 2 + (lane >> 3)) * 8 + j) * 64 + 8 * cw + (lane & 7)) * 16 : OOB);
    }
    {
      int sp = 0; while (ctl[32] < cw * 8 + 8 && ++sp < SPIN_CAP) __builtin_amdgcn_s_sleep(1);
      FENCE();
      const LAS u32x4* sl = (const LAS u32x4*)(L + cw * 8192) + lane;
#pragma unroll
      for (int t = 0; t < 8; ++t) kk[t] = sl[t * 64];
      LGKM0(); ctl[8 + cw] = 1;
    }
#define PULL2() do { if (npre == 1 && __builtin_amdgcn_readfirstlane(ctl[32]) >= cw * 8 + 72) { FENCE(); \
      const LAS u32x4* sl = (const LAS u32x4*)(L + (cw + 8) * 8192) + lane; \
      _Pragma("unroll") for (int t = 0; t < 8; ++t) FB[t] = sl[t * 64];        \
      LGKM0(); ctl[8 + cw] = 2; npre = 2; } } while (0)
    {
      const int o = WS_QP + (((h * 2 + b) * 8 + (lane >> 3)) * 64 + 8 * cw + (lane & 7)) * 16;
      u32x4 v; int sp = 0;
      while (true) {
        FENCE();
        v = ld16(rt, o);
        if (__all(v[1] == tagA && v[3] == tagB) || ++sp > SPIN_CAP) break;
        PULL2();
      }
      float q0 = asf(v[0]), q1 = asf(v[2]);
      q0 += dpp<R8>(q0); q1 += dpp<R8>(q1);
      q0 = sw16_add(q0, q0); q1 = sw16_add(q1, q1); q0 = sw32_add(q0, q0); q1 = sw32_add(q1, q1);
      if (lane < 8) *(volatile LAS unsigned*)(L + L_Q + (8 * cw + lane) * 4) = nosent(pk(q0, q1));
    }
    { int sp = 0;
      while (true) { FENCE(); qv = *(const LAS u32x4*)(L + L_Q + col * 16); if (__all(mx4(qv) != SENT) || ++sp > SPIN_CAP) break; PULL2(); } }
  }

  float sc[8], o8[8], scale = 0.f;
#pragma unroll
  for (int k = 0; k < 8; ++k) { sc[k] = 0.f; o8[k] = 0.f; }
#define WAIT_LANDED(thr, SL) do { if (have < (thr)) { int sp_ = 0; while ((have = __builtin_amdgcn_readfirstlane(ctl[32])) < (thr) && ++sp_ < SPIN_CAP) __builtin_amdgcn_s_sleep(SL); } FENCE(); } while (0)
#define SCORES(res) do { float d[8], u[4], v[2]; \
    _Pragma("unroll") for (int t = 0; t < 8; ++t) { d[t] = 0.f; _Pragma("unroll") for (int k = 0; k < 4; ++k) d[t] = dot2(kk[t][k], qv[k], d[t]); } \
    _Pragma("unroll") for (int k = 0; k < 4; ++k) { const float p0 = d[k] + dpp<R8>(d[k]), p1 = d[k + 4] + dpp<R8>(d[k + 4]); u[k] = (lane & 8) ? p1 : p0; } \
    _Pragma("unroll") for (int k = 0; k < 2; ++k) { const float p0 = u[k] + dpp<X7>(u[k]), p1 = u[k + 2] + dpp<X7>(u[k + 2]); v[k] = (lane & 4) ? p1 : p0; } \
    const float p0 = v[0] + dpp<X2>(v[0]), p1 = v[1] + dpp<X2>(v[1]), y_ = (lane & 2) ? p1 : p0; res = y_ + dpp<X1>(y_); } while (0)
#define PV(t) do { const float pt = dpp<0x150 + 2 * t>(sc[0]); _Pragma("unroll") for (int k = 0; k < 4; ++k) { o8[2 * k] = fmaf(pt, lo(kk[t][k]), o8[2 * k]); o8[2 * k + 1] = fmaf(pt, hi(kk[t][k]), o8[2 * k + 1]); } } while (0)
#define PINO() asm volatile("" : "+v"(o8[0]), "+v"(o8[1]), "+v"(o8[2]), "+v"(o8[3]), "+v"(o8[4]), "+v"(o8[5]), "+v"(o8[6]), "+v"(o8[7]) :: "memory")
  int have = -OOB;
#pragma unroll 1
  for (int n = 0; n < npre; ++n) {
    float y; SCORES(y);
#pragma unroll
    for (int k = 0; k < 7; ++k) sc[k] = sc[k + 1];
    sc[7] = y;
#pragma unroll
    for (int t = 0; t < 8; ++t) kk[t] = FB[t];
  }
  unsigned off = ((npre * 8 + cw) % NSLOT) * 8192;
#pragma unroll 1
  for (int n = npre; n < 16; ++n) {
    const LAS u32x4* sl = (const LAS u32x4*)(L + off) + lane;
    off += 8 * 8192; if (off >= RING) off -= RING;
    const int n0 = (n * 8 + cw) * 8;
    float y = 0.f;
    if (n < 8) {
      WAIT_LANDED(n0 + 8, 1);
#pragma unroll
      for (int t = 0; t < 8; ++t) kk[t] = sl[t * 64];
      LGKM0(); ctl[8 + cw] = n + 1;
      asm volatile("" : "+v"(kk[0]), "+v"(kk[1]), "+v"(kk[2]), "+v"(kk[3]), "+v"(kk[4]), "+v"(kk[5]), "+v"(kk[6]), "+v"(kk[7]) :: "memory");
      SCORES(y);
    } else {
      const int ta = n < 15 ? n0 + 4 : 964 + 4 * cw, tb = n < 15 ? n0 + 5 : 993 + cw, ts = n < 15 ? 1 : 8;
      WAIT_LANDED(ta, 1);
#pragma unroll
      for (int t = 0; t < 4; ++t) kk[t] = sl[t * 64];
      PV(0); PV(1);
      PINO();
      WAIT_LANDED(tb, 0); kk[4] = sl[256];
      PV(2); PV(3);
      PINO();
      WAIT_LANDED(tb + ts, 0); kk[5] = sl[320];
      PV(4);
      PINO();
      WAIT_LANDED(tb + 2 * ts, 0); kk[6] = sl[384];
      PV(5); PV(6);
      PINO();
      WAIT_LANDED(tb + 3 * ts, 0); kk[7] = sl[448];
      LGKM0(); ctl[8 + cw] = n + 1; PIN(kk[7]);
      PV(7);
      if (n == 15) break;
    }
#pragma unroll
    for (int k = 0; k < 7; ++k) sc[k] = sc[k + 1];
    sc[7] = y;
    if (n == 7) {
      float mx = -INFINITY;
#pragma unroll
      for (int k = 0; k < 8; ++k) { sc[k] = rb(rb(sc[k]) * 0.08838834764831845f); mx = fmaxf(mx, sc[k]); }
      mx = fmaxf(mx, dpp<R8>(mx)); mx = fmaxf(mx, dpp<R4>(mx)); mx = fmaxf(mx, dpp<R2>(mx));
      mx = sw32_max(sw16_max(mx));
      *(volatile LAS unsigned*)(L + L_Q + 256 + cw * 4) = nosent(asu(mx));
      u32x4 t0, t1; int sp = 0;
      while (true) { FENCE(); t0 = *(const LAS u32x4*)(L + L_Q + 256); t1 = *(const LAS u32x4*)(L + L_Q + 272); if (__all(umax(mx4(t0), mx4(t1)) != SENT) || ++sp > SPIN_CAP) break; __builtin_amdgcn_s_sleep(1); }
      mx = fmaxf(fmaxf(fmaxf(asf(t0[0]), asf(t0[1])), fmaxf(asf(t0[2]), asf(t0[3]))), fmaxf(fmaxf(asf(t1[0]), asf(t1[1])), fmaxf(asf(t1[2]), asf(t1[3]))));
      const float mneg = -mx * L2E; float l = 0.f;
#pragma unroll
      for (int k = 0; k < 8; ++k) { sc[k] = ex2(fmaf(sc[k], L2E, mneg)); l += sc[k]; }
      *(volatile LAS unsigned*)(L + L_Q + 288 + cw * 4) = nosent(asu(0.5f * wave_sum(l)));
    }
    if (n >= WN0 && n < WN0 + wsteps) {
      const u32x4 t = __builtin_amdgcn_raw_buffer_load_b128(mk((n & 1) ? wpb : wpa, wsz), woff, 0, 0);
      woff += (n & 1) ? wstepb : wstepa;
      *(LAS u32x4*)(scr + (n & 1) * 1024 + lane * 16) = t;
      if (n & 1) {
        LGKM0();
        const u32x2 a0 = __builtin_bit_cast(u32x2, __builtin_amdgcn_ds_read_tr16_b64_v4i16((LAS s16x4*)(scr + ta0))), a1 = __builtin_bit_cast(u32x2, __builtin_amdgcn_ds_read_tr16_b64_v4i16((LAS s16x4*)(scr + ta0 + 4 * trow)));
        const u32x2 c0 = __builtin_bit_cast(u32x2, __builtin_amdgcn_ds_read_tr16_b64_v4i16((LAS s16x4*)(scr + ta1))), c1 = __builtin_bit_cast(u32x2, __builtin_amdgcn_ds_read_tr16_b64_v4i16((LAS s16x4*)(scr + ta1 + 4 * trow)));
#pragma unroll
        for (int i = 0; i < 6; ++i) FB[i] = FB[i + 2];
        FB[6] = u32x4{a0[0], a0[1], a1[0], a1[1]}; FB[7] = u32x4{c0[0], c0[1], c1[0], c1[1]};
        LGKM0();
      }
    }
    if (n == 13) {
      *LP(u32x2, l_scr) = u32x2{SENT, SENT};
      if (lane < 36) *(LAS u32x4*)(scr + 1024 + lane * 16) = (lane >= 24 && lane < 24 + gu_nk / 8) ? u32x4{0x3f803f80u, 0x3f803f80u, 0x3f803f80u, 0x3f803f80u} : u32x4{0, 0, 0, 0};
      FENCE(); ctl[24 + cw] = 1;
    }
    if (n == 14 && cw == 7) {
      int sp = 0;
      while (true) { FENCE(); const u32x4 f0 = *(const LAS u32x4*)(L + L_CTL + 96), f1 = *(const LAS u32x4*)(L + L_CTL + 112);
        if (__all(umin(umin(umin(f0[0], f0[1]), umin(f0[2], f0[3])), umin(umin(f1[0], f1[1]), umin(f1[2], (unsigned)ctl[2]))) != 0 && *(volatile LAS unsigned*)(L + L_Q + 320) != SENT) || ++sp > SPIN_CAP) break;
        __builtin_amdgcn_s_sleep(2); }
      scale = *(const LAS float*)(L + L_Q + 320); PIN(scale);
    }
  }
  {
    float t[4], r[2];
#pragma unroll
    for (int k = 0; k < 4; ++k) t[k] = sw16_add(o8[k], o8[k + 4]);
#pragma unroll
    for (int k = 0; k < 2; ++k) r[k] = sw32_add(t[k], t[k + 2]);
    if (cw != 7) *LP(u32x2, l_scr) = u32x2{nosent(asu(r[0])), nosent(asu(r[1]))};
    else {
      u32x2 s[7]; int sp = 0;
      while (true) { FENCE(); unsigned worst = 0;
#pragma unroll
        for (int k = 0; k < 7; ++k) { s[k] = *(const LAS u32x2*)(L + L_SCR + k * 2048 + lane * 8); worst = umax(worst, umax(s[k][0], s[k][1])); }
        if (__all(worst != SENT) || ++sp > SPIN_CAP) break; }
      r[0] += ((asf(s[0][0]) + asf(s[1][0])) + (asf(s[2][0]) + asf(s[3][0]))) + ((asf(s[4][0]) + asf(s[5][0])) + asf(s[6][0]));
      r[1] += ((asf(s[0][1]) + asf(s[1][1])) + (asf(s[2][1]) + asf(s[3][1]))) + ((asf(s[4][1]) + asf(s[5][1])) + asf(s[6][1]));
      const unsigned v = nosent(pk(r[0] * scale, r[1] * scale));
      auto y = __builtin_amdgcn_permlane32_swap(v, v, false, false);
      auto z0 = __builtin_amdgcn_permlane16_swap(y[0], y[0], false, false), z1 = __builtin_amdgcn_permlane16_swap(y[1], y[1], false, false);
      st16(u32x4{z0[0], z1[0], z0[1], z1[1]}, rt, a_rec);
    }
  }
  BAR();
  if (!r_wo) {
    u32x4 T[6];
#pragma unroll
    for (int i = 0; i < 6; ++i) { T[i] = __builtin_amdgcn_raw_buffer_load_b128(mk((i & 1) ? wpb : wpa, wsz), woff, 0, 0); woff += (i & 1) ? wstepb : wstepa; }
    LAS char* sc2 = L + 8192 + cw * 8192;
#pragma unroll
    for (int i = 0; i < 6; ++i) *(LAS u32x4*)(sc2 + i * 1024 + lane * 16) = T[i];
    FENCE();
    FB[0] = FB[6]; FB[1] = FB[7];
#pragma unroll
    for (int i = 0; i < 6; ++i) {
      const u32x2 a0 = __builtin_bit_cast(u32x2, __builtin_amdgcn_ds_read_tr16_b64_v4i16((LAS s16x4*)(sc2 + i * 1024 + ta0))), a1 = __builtin_bit_cast(u32x2, __builtin_amdgcn_ds_read_tr16_b64_v4i16((LAS s16x4*)(sc2 + i * 1024 + ta0 + 128)));
      FB[2 + i] = u32x4{a0[0], a0[1], a1[0], a1[1]}; }
  }
  if (r_wo) {
    u32x4 B0, B1; int sp = 0;
    while (true) {
      FENCE(); B0 = ld16(rt, a_p); B1 = ld16(rt, a_p + 128);
      if (__all(umax(mx4(B0), mx4(B1)) != SENT) || ++sp > SPIN_CAP) break;
      __builtin_amdgcn_s_sleep(SLP);
    }
    const f32x4 z = {0.f, 0.f, 0.f, 0.f};
    const f32x4 c0 = mfma(FB[4], B1, mfma(FB[0], B0, z)), c1 = mfma(FB[5], B1, mfma(FB[1], B0, z)), c2 = mfma(FB[6], B1, mfma(FB[2], B0, z)), c3 = mfma(FB[7], B1, mfma(FB[3], B0, z));
    f32x4 c;
#pragma unroll
    for (int r = 0; r < 4; ++r) { const float t0 = col < 8 ? c0[r] : c2[r], t1 = col < 8 ? c1[r] : c3[r];
      const float v0 = t0 + dpp<X1>(t0), v1 = t1 + dpp<X1>(t1); float v = (col & 1) ? v1 : v0; v += dpp<X2>(v); c[r] = v + dpp<R8>(v); }
    if ((col & 10) == 0) *LP(f32x4, l_w) = c;
    BAR();
    if (cw == 0) {
      const f32x4 s = *LP(f32x4, l_r) + *LP(f32x4, l_r + 256);
      const float t0 = sw16_add(s[0], s[1]), t1 = sw16_add(s[2], s[3]);
      auto y0 = __builtin_amdgcn_permlane32_swap(asu(t0), asu(t0), false, false), y1 = __builtin_amdgcn_permlane32_swap(asu(t1), asu(t1), false, false);
      const unsigned q0 = nosent(asu(asf(y0[0]) + asf(y0[1]))), q1 = nosent(asu(asf(y1[0]) + asf(y1[1])));
      auto z0 = __builtin_amdgcn_permlane16_swap(q0, q0, false, false), z1 = __builtin_amdgcn_permlane16_swap(q1, q1, false, false);
      st16(u32x4{z0[0], z0[1], z1[0], z1[1]}, rt, a_s);
    }
  } else if (r_gu) {
    const u32x2 xv = *LP(u32x2, l_x);
    u32x4 P[4]; int sp = 0;
    while (true) {
      FENCE();
#pragma unroll
      for (int i = 0; i < 4; ++i) P[i] = __builtin_amdgcn_raw_buffer_load_b128(rt, a_p, i * 2 * D * 4, AUX_SC1);
      if (__all(umax(umax(mx4(P[0]), mx4(P[1])), umax(mx4(P[2]), mx4(P[3]))) != SENT) || ++sp > SPIN_CAP) break;
      __builtin_amdgcn_s_sleep(SLP);
    }
    float hv[4];
#pragma unroll
    for (int k = 0; k < 4; ++k) { const float s = (asf(P[0][k]) + asf(P[1][k])) + (asf(P[2][k]) + asf(P[3][k]));
      hv[k] = ((k & 1) ? hi(xv[k >> 1]) : lo(xv[k >> 1])) + rb(s); }
    *LP(u32x2, a_w) = u32x2{pk(hv[0], hv[1]), pk(hv[2], hv[3])};
    FENCE();
    const u32x4 h0 = *LP(u32x4, l_h), h1 = *LP(u32x4, l_h + 64), h2 = *LP(u32x4, l_h + 128);
    const f32x4 z = {0.f, 0.f, 0.f, 0.f};
    const f32x4 g0 = mfma(FB[0], h0, z), u0 = mfma(FB[1], h0, z), s0 = mfma(h0, h0, z), g2 = mfma(FB[4], h2, z), u2 = mfma(FB[5], h2, z), s2_ = mfma(h2, h2, z);
    const f32x4 cg = mfma(FB[2], h1, g0) + g2, cu = mfma(FB[3], h1, u0) + u2, st = mfma(h1, h1, s0) + s2_;
    if (col < 3) {
#pragma unroll
      for (int r = 0; r < 4; ++r) { *LP(float, l_w + r * 48) = cg[r]; *LP(float, l_w + 2400 + r * 48) = cu[r]; }
    }
    if (lane < 2) { *(LAS float*)(L + L_RED + 4800 + lane * 64 + cw * 4) = lane ? st[1] : st[0]; *(LAS float*)(L + L_RED + 4832 + lane * 64 + cw * 4) = st[2]; }
    BAR();
    if (cw == 0) {
      const LAS f32x4* sp_ = (const LAS f32x4*)(L + L_RED + 4800 + (kq & 1) * 64);
      const f32x4 q2 = sp_[0] + sp_[1], q1 = sp_[2] + sp_[3], a_ = *LP(f32x4, l_r) + *LP(f32x4, l_r + 16), c_ = *LP(f32x4, l_s) + *LP(f32x4, l_s + 16);
      const float s1 = (q1[0] + q1[1]) + (q1[2] + q1[3]), s2 = (q2[0] + q2[1]) + (q2[2] + q2[3]), s = (a_[0] + a_[1]) + (a_[2] + a_[3]), cs = (c_[0] + c_[1]) + (c_[2] + c_[3]);
      const float mean = s1 * (1.f / D), var = fmaxf(fmaf(-mean, mean, s2 * (1.f / D)), 0.f);
      const float den = rb(__builtin_amdgcn_sqrtf(rb(rb(var) + 1e-5f)));
      const float y = rb(fmaf(-rb(mean), cs, s) * rcp(den));
      auto r = __builtin_amdgcn_permlane32_swap(asu(y), asu(y), false, false);
      const float gt = asf(r[0]), up = asf(r[1]);
      const float e = ex2(gt * fmaf(gt * gt, -2.f * L2E * 0.7978845608028654f * 0.044715f, -2.f * L2E * 0.7978845608028654f));
      const float gv = rb(gt * rcp(1.f + e)) * up;
      const unsigned v = nosent(pk(gv, dpp<X1>(gv)));
      st16(u32x4{v, dppu<0x102>(v), dppu<0x104>(v), dppu<0x106>(v)}, rt, a_s);
    }
  } else if (r_dn) {
    const unsigned xv = *LP(u16, l_x);
    u32x4 B[7]; unsigned P[4]; int sp = 0;
    while (true) {
      FENCE();
#pragma unroll
      for (int i = 0; i < 4; ++i) P[i] = __builtin_amdgcn_raw_buffer_load_b32(rt, a_w, i * 2 * D * 4, AUX_SC1);
      if (__all(umax(umax(P[0], P[1]), umax(P[2], P[3])) != SENT) || ++sp > SPIN_CAP) break;
      __builtin_amdgcn_s_sleep(SLP);
    }
    float hres = rb(lo(xv) + rb((asf(P[0]) + asf(P[1])) + (asf(P[2]) + asf(P[3])))); PIN(hres);
    BAR();
    sp = 0;
    u32x4 gb;
    while (true) {
      FENCE(); gb = ld16(rt, a_p);
      if (__all(mx4(gb) != SENT) || ++sp > SPIN_CAP) break;
      __builtin_amdgcn_s_sleep(SLP);
    }
    *(LAS u32x4*)(scr + 1024 + lane * 16) = gb;
    FENCE();
#pragma unroll
    for (int i = 0; i < 7; ++i) B[i] = *LP(u32x4, l_h + i * 32);
    f32x4 c0 = {0.f, 0.f, 0.f, 0.f}, c1 = c0, c2 = c0, c3 = c0;
    c0 = mfma(FB[0], B[0], c0); c1 = mfma(FB[1], B[1], c1); c2 = mfma(FB[2], B[2], c2); c3 = mfma(FB[3], B[3], c3);
    c0 = mfma(FB[4], B[4], c0); c1 = mfma(FB[5], B[5], c1); c2 = mfma(FB[6], B[6], c2);
    if (col < 2) *LP(f32x4, l_w) = (c0 + c1) + (c2 + c3);
    BAR();
    if (cw == 0) {
      float s = (*LP(float, l_r) + *LP(float, l_r + 256)) + (*LP(float, l_r + 512) + *LP(float, l_r + 768));
      s = sw32_add(s, s);
      const float y = hres + rb(s);
      __builtin_amdgcn_raw_buffer_store_b32(pk(y, dpp<X1>(y)), mk(out, 2 * D * 2), a_s, 0, 0);
      if (dn_cg == 0) __builtin_amdgcn_raw_buffer_store_b64(u32x2{tagA, tagB}, rt, lane == 0 ? WS_EPOCH : OOB, 0, AUX_SC1);
    }
  }
  __builtin_amdgcn_s_waitcnt(0x0f70);
}

extern "C" void kernel_launch(void* const* d_in, const int* in_sizes, int n_in, void* d_out, int out_size, void* d_ws, size_t ws_size, hipStream_t stream) {
  (void)hipFuncSetAttribute((const void*)k_block, hipFuncAttributeMaxDynamicSharedMemorySize, L_END);
  hipLaunchKernelGGL(k_block, dim3(NWG), dim3(NTHR), L_END, stream, (const u16*)d_in[0], (const char*)d_in[1], (const char*)d_in[2], (const u16*)d_in[3], (const u16*)d_in[4],
                     (const u16*)d_in[6], (const u16*)d_in[7], (const u16*)d_in[8], (u16*)d_out, (char*)d_ws);
}
```
